```python
import torch

def _softmax(x, axis=-1):
    return torch.softmax(x, dim=axis)

def _gelu(x):
    return torch.nn.functional.gelu(x, approximate='tanh')

def _silu(x):
    return torch.nn.functional.silu(x)

def _sigmoid(x):
    return torch.sigmoid(x)

def _layernorm(x, axis=-1, eps=1e-05):
    m = x.mean(dim=axis, keepdim=True)
    v = x.var(dim=axis, keepdim=True, unbiased=False)
    return (x - m) / torch.sqrt(v + eps)

def _scatter_add(tgt, idx, src, axis=0):
    return tgt.clone().index_add_(axis, idx, src)

def _segment_sum(src, seg, n):
    out = torch.zeros((n,) + src.shape[1:], dtype=src.dtype)
    return out.index_add_(0, seg, src)

def _pad(x, pw):
    flat = tuple((v for pair in reversed(pw) for v in pair))
    return torch.nn.functional.pad(x, flat)
kernel_name = 'standalone__varlen_attn_packed__ragged'

def setup_inputs():

    _w = lambda a: (lambda t: t.to(torch.bfloat16) if t.is_floating_point() else t)(torch.as_tensor(a))
    cu = torch.tensor([0, 828, 1660, 3299, 3744], dtype=torch.int64)
    T, H, D = (int(cu[-1]), 16, 128)
    return {'q': _w((lambda *s: torch.randn(*s, dtype=torch.bfloat16) * 0.8)(T, H, D)), 'k': _w((lambda *s: torch.randn(*s, dtype=torch.bfloat16) * 0.8)(T, H, D)), 'v': _w((lambda *s: torch.randn(*s, dtype=torch.bfloat16) * 0.05)(T, H, D)), 'cu': _w(cu)}

def reference(q, k, v, cu):
    T, H, D = q.shape
    out = torch.zeros_like(q)
    scale = D ** (-0.5)
    outs = []
    for i in range(len(cu) - 1):
        s, e = (int(cu[i]), int(cu[i + 1]))
        qi, ki, vi = (q[s:e].clone(), k[s:e].clone(), v[s:e].clone())
        p = _softmax(torch.einsum('qhd,khd->hqk', qi, ki) * scale, axis=-1)
        outs.append(torch.einsum('hqk,khd->qhd', p, vi))
    return torch.concatenate(outs, axis=0)
```

```cpp
#include <hip/hip_runtime.h>
#include <hip/hip_bf16.h>
#include <cstdio>
using bf16 = __hip_bfloat16;
constexpr int D = 128, H = 16, NW = 8, QBLK = 32, KVBLK = 64, LD = H * D;
constexpr float SCALE = 0.088388347648318440f;
constexpr size_t SHM_V = KVBLK * D * 2, SHM_K = KVBLK * D * 2, SHM_ATTN = 2 * SHM_V + 2 * SHM_K + NW * 64 * 4;
using bf16x8 = __attribute__((ext_vector_type(8))) short;
using s16x4  = __attribute__((ext_vector_type(4))) short;
using f32x16 = __attribute__((ext_vector_type(16))) float;
using u32x4  = __attribute__((ext_vector_type(4))) unsigned;
#define KSWZ(row, colB) ((row) * 256 + ((colB) ^ (((row) & 7) << 4)))
#define SBAR() __builtin_amdgcn_sched_barrier(0)
__device__ __forceinline__ int crow(int r, int hi) { return (r & 3) + 8 * (r >> 2) + 4 * hi; }
__device__ __forceinline__ unsigned cvtpk(float lo, float hi) {
  unsigned r; asm volatile("v_cvt_pk_bf16_f32 %0, %1, %2" : "=v"(r) : "v"(lo), "v"(hi)); return r;
}
__device__ __forceinline__ void partialSM(f32x16& p0, f32x16& p1, float& m_reg, float& mn, float& alpha) {
  constexpr float C = SCALE * 1.4426950408889634f;
  float pmax = p0[0]; for (int r = 1; r < 16; ++r) pmax = fmaxf(pmax, p0[r]); for (int r = 0; r < 16; ++r) pmax = fmaxf(pmax, p1[r]);
  { auto rr = __builtin_amdgcn_permlane32_swap(__float_as_uint(pmax), __float_as_uint(pmax), false, false);
    pmax = fmaxf(__uint_as_float(rr[0]), __uint_as_float(rr[1])); }
  if (__builtin_expect(__all(pmax - m_reg <= 8.f / SCALE), 1)) { mn = m_reg; alpha = 1.f; }
  else { mn = fmaxf(m_reg, pmax); alpha = __builtin_amdgcn_exp2f((m_reg - mn) * C); m_reg = mn; }
  float mnC = -mn * C;
  for (int r = 0; r < 16; ++r) p0[r] = fmaf(p0[r], C, mnC); for (int r = 0; r < 16; ++r) p1[r] = fmaf(p1[r], C, mnC);
  for (int r = 0; r < 16; ++r) p0[r] = __builtin_amdgcn_exp2f(p0[r]);
}
__device__ __forceinline__ void finishSM(f32x16& p0, f32x16& p1, float alpha, float& l_reg, bf16x8& pa0, bf16x8& pa1, bf16x8& pa2, bf16x8& pa3) {
  for (int r = 0; r < 16; ++r) p1[r] = __builtin_amdgcn_exp2f(p1[r]);
  float ps = 0; for (int r = 0; r < 16; ++r) ps += p0[r]; for (int r = 0; r < 16; ++r) ps += p1[r];
  { auto rr = __builtin_amdgcn_permlane32_swap(__float_as_uint(ps), __float_as_uint(ps), false, false);
    ps = __uint_as_float(rr[0]) + __uint_as_float(rr[1]); }
  l_reg = l_reg * alpha + ps;
#define PK4(P, BASE, OUT) do { unsigned a0 = cvtpk(P[BASE + 0], P[BASE + 1]), a1 = cvtpk(P[BASE + 2], P[BASE + 3]);   \
    unsigned b0 = cvtpk(P[BASE + 4], P[BASE + 5]), b1 = cvtpk(P[BASE + 6], P[BASE + 7]);                              \
    auto r0 = __builtin_amdgcn_permlane32_swap(a0, b0, false, false); auto r1 = __builtin_amdgcn_permlane32_swap(a1, b1, false, false); \
    u32x4 w = {r0[0], r1[0], r0[1], r1[1]}; OUT = *reinterpret_cast<bf16x8*>(&w); } while (0)
  PK4(p0, 0, pa0); PK4(p0, 8, pa1); PK4(p1, 0, pa2); PK4(p1, 8, pa3);
#undef PK4
}
template <int KB>
__device__ __forceinline__ void qkt(f32x16& p0, f32x16& p1, const char* K_lds, int r32, int hi, const bf16x8* qr) {
    p0 = f32x16{}; p1 = f32x16{};
    const char* kb[4];
#pragma unroll
    for (int dd = 0; dd < 4; ++dd) kb[dd] = K_lds + KB * SHM_K + KSWZ(r32, (dd * 16 + hi * 8) * 2);
#pragma unroll
    for (int d0 = 0; d0 < 8; ++d0) { const char* a = kb[d0 & 3] + (d0 >> 2) * 128;
        bf16x8 b0 = *reinterpret_cast<const bf16x8*>(a);
        bf16x8 b1 = *reinterpret_cast<const bf16x8*>(a + 32 * 256);
        p0 = __builtin_amdgcn_mfma_f32_32x32x16_bf16(b0, qr[d0], p0, 0, 0, 0);
        p1 = __builtin_amdgcn_mfma_f32_32x32x16_bf16(b1, qr[d0], p1, 0, 0, 0); }
}
__device__ __forceinline__ int v_st(int k, int c) { const int kk = (k & ~0xC) | ((k & 4) << 1) | ((k & 8) >> 1); return ((kk >> 3) * 4 + (c >> 5)) * 512 + ((kk & 7) * 32 + (c & 31)) * 2; }
__device__ __forceinline__ int v_rd_base(int lane) { return ((lane & 3) << 3) | (((lane >> 2) & 3) << 6) | (((lane >> 4) & 1) << 5) | (((lane >> 5) & 1) << 8); }
constexpr int v_rd_off(int d0, int ks, int half) { return d0 * 512 + ks * 4096 + half * 2048; }
template <int VB>
__device__ __forceinline__ void pv_tile(f32x16* o, int vb0, bf16x8 pa0, bf16x8 pa1, bf16x8 pa2, bf16x8 pa3) {
#define TRRD(dst, off) asm volatile("ds_read_b64_tr_b16 %0, %1 offset:%2" : "=&v"(dst) : "v"(vb0), "i"(off) : "memory")
#define PV_D0(d0) do { s16x4 l0, l1, l2, l3, h0, h1, h2, h3; constexpr int b_ = VB * SHM_V + v_rd_off(d0, 0, 0); \
        TRRD(l0, b_); TRRD(h0, b_ + 2048); TRRD(l1, b_ + 4096); TRRD(h1, b_ + 6144); TRRD(l2, b_ + 8192); TRRD(h2, b_ + 10240); TRRD(l3, b_ + 12288); TRRD(h3, b_ + 14336); \
        asm volatile("s_waitcnt lgkmcnt(0)" ::: "memory"); SBAR(); \
        o[d0] = __builtin_amdgcn_mfma_f32_32x32x16_bf16(pa0, (bf16x8){l0[0], l0[1], l0[2], l0[3], h0[0], h0[1], h0[2], h0[3]}, o[d0], 0, 0, 0);   \
        o[d0] = __builtin_amdgcn_mfma_f32_32x32x16_bf16(pa1, (bf16x8){l1[0], l1[1], l1[2], l1[3], h1[0], h1[1], h1[2], h1[3]}, o[d0], 0, 0, 0);   \
        o[d0] = __builtin_amdgcn_mfma_f32_32x32x16_bf16(pa2, (bf16x8){l2[0], l2[1], l2[2], l2[3], h2[0], h2[1], h2[2], h2[3]}, o[d0], 0, 0, 0);   \
        o[d0] = __builtin_amdgcn_mfma_f32_32x32x16_bf16(pa3, (bf16x8){l3[0], l3[1], l3[2], l3[3], h3[0], h3[1], h3[2], h3[3]}, o[d0], 0, 0, 0); } while (0)
    PV_D0(0); PV_D0(1); PV_D0(2); PV_D0(3);
#undef PV_D0
#undef TRRD
}
__device__ __forceinline__ void mask_tile(f32x16& p0, f32x16& p1, int nvalid  ) {
    const float NEG = -__builtin_inff();
#pragma unroll
    for (int r = 0; r < 16; ++r) { const int c = (r & 3) + 8 * (r >> 2);
        if (c >= nvalid) p0[r] = NEG; if (c + 32 >= nvalid) p1[r] = NEG; }
}
__global__ __launch_bounds__(512, 1)
void attn_v0(const bf16* __restrict__ Q, const bf16* __restrict__ K, const bf16* __restrict__ V, const int* __restrict__ cu, bf16* __restrict__ O, int nseq) {
  extern __shared__ __attribute__((aligned(16))) char lds[];
  const int h = blockIdx.x & 15; int j = blockIdx.x >> 4;
  int s0 = 0, s1 = 0; bool found = false;
  for (int i = 0; i < nseq; ++i) { const int a = cu[i], b = cu[i + 1]; const int nq = (b - a + 255) >> 8;
    if (!found) { if (j < nq) { s0 = a; s1 = b; found = true; } else j -= nq; } }
  if (!found) return;
  const int L = s1 - s0, NT = (L + 63) >> 6, q0 = j * 256;
  const int tid = threadIdx.x, wid = __builtin_amdgcn_readfirstlane(tid >> 6), lane = tid & 63, r32 = lane & 31, hi = lane >> 5;
  char* V_lds = lds; char* K_lds = lds + 2 * SHM_V;
  float* ws = (float*)(lds + 2 * SHM_V + 2 * SHM_K) + wid * 64; float* li_l = ws; float* al_l = ws + 32;
  float m_reg = -1e30f, l_reg = 0; f32x16 o[4] = {}; bf16x8 qr[8];
  const bf16* Kh = K + (size_t)s0 * LD + h * D; const bf16* Vh = V + (size_t)s0 * LD + h * D;
  { int qrow = q0 + wid * QBLK + r32; qrow = qrow < L ? qrow : L - 1;
    const bf16* Qw = Q + (size_t)(s0 + qrow) * LD + h * D + hi * 8;
#pragma unroll
    for (int d0 = 0; d0 < 8; ++d0) qr[d0] = *(const bf16x8*)(Qw + d0 * 16); }
  const int sr = tid >> 4, sc = (tid & 15) * 8, vst0 = v_st(sr, sc), vst1 = v_st(32 + sr, sc), kws = KSWZ(sr, sc * 2);
  const int vb0 = (int)(uintptr_t)V_lds + v_rd_base(lane);
  bf16x8 sv0, sv1, sk0, sk1;
#define CL(r) ((r) < L ? (r) : L - 1)
#define SLOAD(k0) do { const size_t ra = (size_t)CL((k0) + sr) * LD + sc, rb = (size_t)CL((k0) + 32 + sr) * LD + sc; \
    sv0 = *(const bf16x8*)(Vh + ra); sv1 = *(const bf16x8*)(Vh + rb); sk0 = *(const bf16x8*)(Kh + ra); sk1 = *(const bf16x8*)(Kh + rb); } while (0)
#define SWRITE(bf) do { *(bf16x8*)(V_lds + (bf) * SHM_V + vst0) = sv0; *(bf16x8*)(V_lds + (bf) * SHM_V + vst1) = sv1; \
    *(bf16x8*)(K_lds + (bf) * SHM_K + kws) = sk0; *(bf16x8*)(K_lds + (bf) * SHM_K + kws + 32 * 256) = sk1; } while (0)
#define VMW() asm volatile("s_waitcnt vmcnt(0)" ::: "memory")
#define RESC(a) do { if (__any((a) < 1.f)) { if (hi == 0) al_l[r32] = (a); asm volatile("s_waitcnt lgkmcnt(0)" ::: "memory"); \
    for (int d_ = 0; d_ < 4; ++d_) for (int r = 0; r < 16; ++r) o[d_][r] *= al_l[crow(r, hi)]; } } while (0)
#define MASKT(P0_, P1_, t) do { if (((t) + 1) * 64 > L) mask_tile(P0_, P1_, L - (t) * 64 - 4 * hi); } while (0)
  f32x16 pA0, pA1, pB0, pB1; float mnA, mnB, alA, alB; bf16x8 pa0, pa1, pa2, pa3;
  SLOAD(0); VMW(); SWRITE(0); __syncthreads();
  if (NT > 1) SLOAD(64);
  SBAR(); qkt<0>(pA0, pA1, K_lds, r32, hi, qr);
  MASKT(pA0, pA1, 0); partialSM(pA0, pA1, m_reg, mnA, alA);
  if (NT > 1) { VMW(); SWRITE(1); }
  __syncthreads();
#define HALF_STEP(PX0, PX1, mnX, alX, PY0, PY1, alY, t, KB, VB, SB) do { \
        SBAR(); qkt<KB>(PX0, PX1, K_lds, r32, hi, qr); \
        finishSM(PY0, PY1, alY, l_reg, pa0, pa1, pa2, pa3); SBAR(); \
        if ((t) + 1 < NT) { SLOAD(((t) + 1) * 64); SBAR(); } \
        pv_tile<VB>(o, vb0, pa0, pa1, pa2, pa3); MASKT(PX0, PX1, (t)); partialSM(PX0, PX1, m_reg, mnX, alX); \
        __syncthreads(); \
        if ((t) + 1 < NT) { VMW(); SWRITE(SB); } \
        RESC(alX); __syncthreads(); } while (0)
  for (int t = 1; t + 1 < NT; t += 2) {
    HALF_STEP(pB0, pB1, mnB, alB, pA0, pA1, alA, t, 1, 0, 0);
    HALF_STEP(pA0, pA1, mnA, alA, pB0, pB1, alB, t + 1, 0, 1, 1);
  }
  const bool even = (NT & 1) == 0;
  if (even) { SBAR(); qkt<1>(pB0, pB1, K_lds, r32, hi, qr); SBAR(); }
  finishSM(pA0, pA1, alA, l_reg, pa0, pa1, pa2, pa3); SBAR();
  pv_tile<0>(o, vb0, pa0, pa1, pa2, pa3);
  if (even) { MASKT(pB0, pB1, NT - 1); partialSM(pB0, pB1, m_reg, mnB, alB); __syncthreads(); RESC(alB);
    finishSM(pB0, pB1, alB, l_reg, pa0, pa1, pa2, pa3); SBAR(); pv_tile<1>(o, vb0, pa0, pa1, pa2, pa3); }
  if (hi == 0) li_l[r32] = l_reg; asm volatile("s_waitcnt lgkmcnt(0)" ::: "memory");
  float rli[16];
#pragma unroll
  for (int r = 0; r < 16; ++r) rli[r] = __builtin_amdgcn_rcpf(li_l[crow(r, hi)]);
  bf16* Ow = O + (size_t)(s0 + q0 + wid * QBLK) * LD + h * D;
#pragma unroll
  for (int r = 0; r < 16; ++r) { const int orow = crow(r, hi);
#pragma unroll
    for (int d0 = 0; d0 < 4; ++d0) { const float v = o[d0][r] * rli[r]; const float vn = __shfl_xor(v, 1);
      if ((r32 & 1) == 0 && q0 + wid * QBLK + orow < L) *(unsigned*)(Ow + (size_t)orow * LD + d0 * 32 + r32) = cvtpk(v, vn); } }
}
extern "C" void kernel_launch(void* const* d_in, const int* in_sizes, int n_in, void* d_out, int out_size, void* d_ws, size_t ws_size, hipStream_t stream) {
  const int T = in_sizes[0] / LD, nseq = in_sizes[3] - 1;
  hipFuncSetAttribute((const void*)attn_v0, hipFuncAttributeMaxDynamicSharedMemorySize, (int)SHM_ATTN);
  const int maxq = T / 256 + nseq;
  hipLaunchKernelGGL(attn_v0, dim3(maxq * 16), dim3(512), SHM_ATTN, stream, (const bf16*)d_in[0], (const bf16*)d_in[1], (const bf16*)d_in[2], (const int*)d_in[3], (bf16*)d_out, nseq);
}
```

```cpp
#include <hip/hip_runtime.h>
#include <hip/hip_bf16.h>
#include <stdint.h>

typedef unsigned u32x4 __attribute__((ext_vector_type(4)));
typedef unsigned u32x2 __attribute__((ext_vector_type(2)));
typedef short bf16x8 __attribute__((ext_vector_type(8)));
typedef short s16x4 __attribute__((ext_vector_type(4)));
typedef float f32x16 __attribute__((ext_vector_type(16)));
extern "C" __device__ unsigned long long amd_dispatch_id() __asm("llvm.amdgcn.dispatch.id");

constexpr int NH = 16, HD = 128, ROWB = NH * HD * 2;
constexpr int GPH = 16;
constexpr unsigned LDS_BYTES = 163840;
constexpr unsigned PSTRIDE = 69888u;
constexpr float CEXP = 0.12751743082f;
#define CQV 0x120f0c18u
#define MINPV 2.f
#define OVFV 0.5f
#define OVV 22
#define GRPV 0
constexpr unsigned CQ = CQV, CF = CQ & 0xffu, OV = OVV, CD = CF * 0x01010101u - CQ, GRP = GRPV; constexpr float MINP = MINPV, OVF = OVFV;

#define DI __device__ __forceinline__
DI unsigned rfl(unsigned x) { return __builtin_amdgcn_readfirstlane(x); }
DI unsigned rdl(unsigned x, unsigned l) { return __builtin_amdgcn_readlane(x, l); }
DI unsigned cvtpk(float lo, float hi) { unsigned r; asm("v_cvt_pk_bf16_f32 %0, %1, %2" : "=v"(r) : "v"(lo), "v"(hi)); return r; }
DI unsigned lane_id() { unsigned x; asm volatile("v_mbcnt_lo_u32_b32 %0, -1, 0\n\tv_mbcnt_hi_u32_b32 %0, -1, %0" : "=v"(x)); return x; }
DI int crow(int r, int h) { return (r & 3) + 8 * (r >> 2) + 4 * h; }

DI void slow_rows(const char* Qh, const char* Kh, const char* Vh, char* Oh, int L, int row0, int rend, char* stage) {
  const unsigned lane = lane_id();
  const int r = lane & 31, h = lane >> 5;
  int qr = row0 + r; const bool qok = qr < L && qr < rend; qr = qr < L ? qr : L - 1;
  bf16x8 q[8];
#pragma unroll
  for (int s = 0; s < 8; ++s) q[s] = *(const bf16x8*)(Qh + (size_t)qr * ROWB + 32 * s + 16 * h);
  float m = -1e30f, l = 0.f; f32x16 o[4] = {};
  const unsigned vb = (unsigned)(uintptr_t)stage + (((lane & 3) << 3) | (((lane >> 2) & 3) << 6) | (((lane >> 4) & 1) << 5) | (h << 8));
  for (int k0 = 0; k0 < L; k0 += 32) {
    int kr = k0 + r; kr = kr < L ? kr : L - 1;
    f32x16 S = {};
#pragma unroll
    for (int s = 0; s < 8; ++s) S = __builtin_amdgcn_mfma_f32_32x32x16_bf16(*(const bf16x8*)(Kh + (size_t)kr * ROWB + 32 * s + 16 * h), q[s], S, 0, 0, 0);
#pragma unroll
    for (int i = 0; i < 8; ++i) { const int n = lane + 64 * i, row = n >> 4, ch = n & 15; u32x4 v = {0, 0, 0, 0};
      if (k0 + row < L) v = *(const u32x4*)(Vh + (size_t)(k0 + row) * ROWB + 16 * ch);
      *(u32x4*)(stage + (row >> 3) * 2048 + (ch >> 2) * 512 + (row & 7) * 64 + (ch & 3) * 16) = v; }
    float pm = -__builtin_inff();
#pragma unroll
    for (int e = 0; e < 16; ++e) { if (k0 + crow(e, h) >= L) S[e] = -__builtin_inff(); pm = fmaxf(pm, S[e]); }
    { auto rr = __builtin_amdgcn_permlane32_swap(__float_as_uint(pm), __float_as_uint(pm), false, false); pm = fmaxf(__uint_as_float(rr[0]), __uint_as_float(rr[1])); }
    const float mn = fmaxf(m, pm), al = __builtin_amdgcn_exp2f((m - mn) * CEXP), nm = -mn * CEXP; m = mn;
    float ps = 0.f;
#pragma unroll
    for (int e = 0; e < 16; ++e) { S[e] = __builtin_amdgcn_exp2f(fmaf(S[e], CEXP, nm)); ps += S[e]; }
    l = l * al + ps;
#pragma unroll
    for (int d = 0; d < 4; ++d)
#pragma unroll
      for (int e = 0; e < 16; ++e) o[d][e] *= al;
    asm volatile("s_waitcnt lgkmcnt(0)" ::: "memory");
#pragma unroll
    for (int ks = 0; ks < 2; ++ks) {
      u32x4 pw = {cvtpk(S[8 * ks], S[8 * ks + 1]), cvtpk(S[8 * ks + 2], S[8 * ks + 3]), cvtpk(S[8 * ks + 4], S[8 * ks + 5]), cvtpk(S[8 * ks + 6], S[8 * ks + 7])};
#pragma unroll
      for (int d = 0; d < 4; ++d) { s16x4 lo, up; const unsigned a = vb + 4096 * ks + 512 * d;
        asm volatile("ds_read_b64_tr_b16 %0, %2\n\tds_read_b64_tr_b16 %1, %2 offset:2048\n\ts_waitcnt lgkmcnt(0)" : "=&v"(lo), "=&v"(up) : "v"(a) : "memory");
        o[d] = __builtin_amdgcn_mfma_f32_32x32x16_bf16((bf16x8){lo[0], lo[1], lo[2], lo[3], up[0], up[1], up[2], up[3]}, __builtin_bit_cast(bf16x8, pw), o[d], 0, 0, 0); }
    }
  }
  { auto rr = __builtin_amdgcn_permlane32_swap(__float_as_uint(l), __float_as_uint(l), false, false); l = __uint_as_float(rr[0]) + __uint_as_float(rr[1]); }
  const float il = 1.f / l;
  if (qok) {
#pragma unroll
    for (int d = 0; d < 4; ++d)
#pragma unroll
      for (int g = 0; g < 4; ++g) { u32x2 wv = {cvtpk(o[d][4 * g] * il, o[d][4 * g + 1] * il), cvtpk(o[d][4 * g + 2] * il, o[d][4 * g + 3] * il)};
        *(u32x2*)(Oh + (size_t)qr * ROWB + 64 * d + 16 * g + 8 * h) = wv; }
  }
}

struct Args { const char* Q; const char* K; const char* V; const int* cu; char* O; char* WS; int nseq; int ntok; int lgw; int pad0;
};
typedef const __attribute__((address_space(4))) Args* ArgsP;
DI char* srd_base(const u32x4& s) { return (char*)(((unsigned long long)s[1] << 32) | s[0]) + 4096; }

DI unsigned mbc(unsigned long long m) { return __builtin_amdgcn_mbcnt_hi((unsigned)(m >> 32), __builtin_amdgcn_mbcnt_lo((unsigned)m, 0u)); }

__global__ __launch_bounds__(256, 1)
void varlen_attn_fwd(Args args_) {
  extern __shared__ __attribute__((aligned(16))) char lds[];
  const ArgsP ka = (ArgsP)__builtin_amdgcn_kernarg_segment_ptr();
  const Args a = {ka->Q, ka->K, ka->V, ka->cu, ka->O, ka->WS, ka->nseq, ka->ntok, ka->lgw, 0};
  unsigned long long pc; asm volatile("s_getpc_b64 %0" : "=s"(pc)); pc &= ~255ull;
  const unsigned w = rfl(threadIdx.x >> 6), l = threadIdx.x & 63, bid = blockIdx.x;
  const unsigned hd = 2 * (bid & 7) + ((bid >> 3) & 1), j = bid >> 4;
  const unsigned nseq = (unsigned)a.nseq;
  const unsigned lgw = (unsigned)a.lgw, sq = l >> lgw, g = l & ((1u << lgw) - 1u);
  unsigned s0, s1;
  { const unsigned o0 = (sq < nseq ? sq : nseq) * 4u, o1 = (sq < nseq ? sq + 1 : nseq) * 4u;
    asm volatile("global_load_dword %0, %2, %4\n\tglobal_load_dword %1, %3, %4" : "=&v"(s0), "=&v"(s1) : "v"(o0), "v"(o1), "s"(a.cu) : "memory"); }
  { const unsigned o = threadIdx.x * 128u;
    asm volatile("global_load_dword v255, %0, %1" :: "v"(o), "s"(pc) : "memory", "v255");
    asm volatile("global_load_dword v255, %0, %1" :: "v"(32768u + (o & 4095u)), "s"(pc) : "memory", "v255"); }
#define CPFN "+2"
  const unsigned r16 = l & 15, g4 = l >> 4;
  unsigned rk0 = 2u * 16384u + 2048 * (r16 >> 3) + 64 * (r16 & 7) + 16 * (g4 ^ ((0u - (r16 >> 2)) & 3));
  unsigned rv0 = 65536u + 2u * 16384u + 2048 * (g4 >> 1) + 256 * (g4 & 1) + 64 * (r16 >> 2) + 16 * (((l & 3) >> 1) ^ ((0u - g4) & 3)) + 8 * (l & 1), rv1 = rv0 ^ 32;
  unsigned dm[4];
#pragma unroll
  for (int pp = 0; pp < 4; ++pp) dm[pp] = (8 * (pp >> 1) + ((l >> 2) & 7)) * ROWB + 64 * (2 * (pp & 1) + (l >> 5)) + 16 * ((l & 3) ^ ((0u - (2 * (pp >> 1) + ((l >> 4) & 1))) & 3)) + 4096 - 1024 * pp;
  unsigned ost = g4 * ROWB + 16 * r16 + 4096, lane16 = l * 16, flagoff = l == 0 ? 0u : 0x40000000u;
  unsigned long long tag = amd_dispatch_id() * 0xD1B54A32D192ED03ull + (unsigned long long)__builtin_amdgcn_dispatch_ptr() * 0x9E3779B97F4A7C16ull + 0x5851F42D4C957F2Dull;
  unsigned wrow = ((w < 2 ? 32u : 0u) + 16u * (w & 1)) * ROWB, stg = w < 2 ? w << 16 : 98304u + (w << 14);
  asm volatile("" : "+v"(rk0), "+v"(rv0), "+v"(rv1), "+v"(dm[0]), "+v"(dm[1]), "+v"(dm[2]), "+v"(dm[3]), "+v"(ost), "+v"(lane16), "+v"(flagoff), "+s"(tag), "+s"(wrow), "+s"(stg));
  const int hb = (int)hd * 256 - 4096;
  auto srd = [](const char* p, unsigned n) { const unsigned long long x = (unsigned long long)p; u32x4 s = {(unsigned)x, (unsigned)(x >> 32) & 0xffffu, n, 0x20000u}; return s; };
  u32x4 srdK = srd(a.K + hb, 0), srdV = srd(a.V + hb, 0), srdQ = srd(a.Q + hb, 0), srdO = srd(a.O + hb, 0), srdW = srd(a.WS, 256u * PSTRIDE);
  asm volatile("s_waitcnt vmcnt(0" CPFN ")" : "+v"(s0), "+v"(s1), "+s"(srdK), "+s"(srdV), "+s"(srdQ), "+s"(srdO), "+s"(srdW) :: "memory");
  const unsigned nk = (s1 - s0 + 63) >> 6, ng = (nk + 3) >> 2, nf = nk >> 2, nq = g < nf ? 4u : (nk & 3u), q0 = 256 * g;
  const float fk = (float)nk, fng = (float)ng, fic = __builtin_fmaf(fk, (float)CF, (float)OV);
  const float fco = __builtin_fmaf(fng, fic, -fk * (float)((CD >> (8 * (nk & 3))) & 0xffu));
  const float c = (float)((CQ >> (8 * (nq & 3))) & 0xffu), r2g = __builtin_amdgcn_rcpf(fng + fng), fg = (float)(2 * g + 1) * r2g, fb = (fng + 0.5f) * r2g;
  float a0 = OVF * OV, ft = 0.f;

  for (unsigned i = 0; i < nseq; ++i) { const unsigned li = i << lgw; const float co = __uint_as_float(rdl(__float_as_uint(fco), li));
    const float cnt = __builtin_floorf(__builtin_fmaf(fg, __uint_as_float(rdl(__float_as_uint(fng), li)), i < sq ? fb : fb - r2g));
    a0 += __builtin_fminf(cnt * __uint_as_float(rdl(__float_as_uint(fic), li)), co); ft += co; }
  const float fG = __builtin_amdgcn_fmed3f(__builtin_floorf(ft * (1.f / 64.f)), 1.f, (float)GPH);
  const float step = ft * __builtin_amdgcn_rcpf(fG), inv = fG * __builtin_amdgcn_rcpf(ft), fj = (float)j;
  const float blo = __builtin_amdgcn_fmed3f(__builtin_ceilf(__builtin_fmaf(__builtin_fminf(MINP - 0.5f, 0.5f * fk), c, a0) * inv), 1.f, fG);
  const float bh1 = __builtin_amdgcn_fmed3f(__builtin_ceilf(__builtin_fmaf(__builtin_fmaxf(fk - (MINP - 0.5f), 0.5f * fk), c, a0) * inv), blo, fG);
  const float rc = __builtin_amdgcn_rcpf(c), x0 = __builtin_fmaf(fj, step, -a0);
  const float ca = __builtin_amdgcn_fmed3f(__builtin_floorf(__builtin_fmaf(x0, rc, 0.5f)), 1.f, fk - 1.f);
  const float cb = __builtin_amdgcn_fmed3f(__builtin_floorf(__builtin_fmaf(__builtin_fmaf(fj + 1.f, step, -a0), rc, 0.5f)), 1.f, fk - 1.f);
  const unsigned ta = (unsigned)(fj < blo ? 0.f : ca), tb = (unsigned)(fj + 2.f > bh1 ? fk : cb);
  const int ib = (int)blo, np = (int)bh1 - ib, me = (int)j - ib + 1, fi = np >= 2 ? 1 : 0;
  const unsigned long long mM = __ballot(g < ng && (unsigned)me <= (unsigned)np), mS = __ballot(np > 0), mE = __ballot(me == fi);
  const unsigned long long mW = mM & ~mS, mF = mM & mS & mE, mP = mM & mS & ~mE;
  const unsigned nP = rfl(__builtin_popcountll(mP)), nWa = rfl(__builtin_popcountll(mW)), nW = rfl(nWa < 7 - nP ? nWa : 7 - nP);
  const bool isP = np > 0 && me != fi, isW = np <= 0;
  const unsigned rP = ta > 0 ? 0u : nP - 1u, rW = nP + mbc(mW), rank = isP ? rP : (isW ? rW : nP + nW);
  unsigned long long rest = mW & __ballot(rW >= nP + nW);
  const unsigned nseg = rfl(nP + nW + (mF ? 1u : 0u)), nseg8 = nseg * 8;
  unsigned tab;
  { const unsigned sb = __umul24(hd * GPH + (unsigned)ib, PSTRIDE >> 8) << 8;
    const u32x4 w0 = {(s0 + q0) * ROWB, s1 * ROWB + 256u, (s0 + 64 * ta) * ROWB, (tb - ta) | (nq << 16)};
    const u32x4 w1 = {s0 * ROWB, tb == nk ? s1 - s0 - 64 * (nk - 1) : 64u, isP ? sb + (__umul24((unsigned)(me > fi ? me - 1 : me), PSTRIDE >> 8) << 8) : 0xffffffffu, (isP || isW) ? 0u : sb | (unsigned)np};
    asm volatile("s_mov_b64 exec, %4\n\tds_write_b128 %1, %2\n\tds_write_b128 %1, %3 offset:16\n\ts_mov_b64 exec, -1\n\tds_read_b32 %0, %5\n\ts_waitcnt lgkmcnt(0)"
                 : "=&v"(tab) : "v"(stg + rank * 32), "v"(w0), "v"(w1), "s"(mM & ~rest), "v"(stg + l * 4) : "memory"); }
  { unsigned mine = 0; for (unsigned n = 0; n < nseg; ++n) { const unsigned x = rdl(tab, n * 8 + 3); mine += (x & 0xffffu) * ((CQ >> (8 * ((x >> 16) & 3))) & 0xffu) + 26u; }
    if (mine < rfl((unsigned)step) + 6u) __builtin_amdgcn_s_sleep(52); }
  unsigned fail = 0;
  if (nseg) {
    asm volatile(
  "s_mov_b32 s71, 0\n"
  "s_mov_b32 s61, 4\n"
  "v_readlane_b32 s40, v228, s61\n"
  "s_add_u32 s58, s71, 1\n"
  "s_add_u32 s59, s71, 2\n"
  "s_add_u32 s60, s71, 3\n"
  "v_readlane_b32 s10, v228, s58\n"
  "v_readlane_b32 s39, v228, s59\n"
  "v_readlane_b32 s47, v228, s60\n"
  "s_add_u32 s58, s71, 8\n"
  "s_and_b32 s47, s47, 0xffff\n"
  "s_lshl_b32 s59, s47, 18\n"
  "s_add_u32 s59, s59, s39\n"
  "s_add_u32 s59, s59, 256\n"
  "s_min_u32 s10, s10, s59\n"
  "s_add_u32 s39, s39, s52\n"
  "s_cmp_ge_u32 s58, s55\n"
  "s_cselect_b32 s47, 0x10000, s47\n"
  "s_lshl_b32 s45, s53, 12\n"
  "s_mov_b32 s43, 16384\n"
  "s_cmp_eq_u32 s53, 1\n"
  "s_cselect_b32 s40, 2146435072, s40\n"
  "s_cmp_ge_u32 s53, 2\n"
  "s_cselect_b32 s40, s39, s40\n"
  "s_cselect_b32 s62, 0x40000, 0\n"
  "s_cselect_b32 s64, 1, 0\n"
  "s_cselect_b32 s41, s39, 2146435072\n"
  "s_or_b32 m0, s45, s43\n"
  "s_nop 0\n"
  "buffer_load_dwordx4 v222, s[8:11], s40 offen offset:0 lds\n"
  "buffer_load_dwordx4 v223, s[8:11], s40 offen offset:1024 lds\n"
  "buffer_load_dwordx4 v224, s[8:11], s40 offen offset:2048 lds\n"
  "buffer_load_dwordx4 v225, s[8:11], s40 offen offset:3072 lds\n"
  "s_add_u32 s58, s71, 0\n"
  "s_add_u32 s59, s71, 1\n"
  "s_add_u32 s61, s71, 3\n"
  "v_readlane_b32 s79, v228, s61\n"
  "v_readlane_b32 s28, v228, s58\n"
  "v_readlane_b32 s18, v228, s59\n"
  "s_lshr_b32 s79, s79, 16\n"
  "s_mul_i32 s61, s53, s79\n"
  "s_lshl_b32 s61, s61, 16\n"
  "s_add_u32 s28, s28, s61\n"
  "s_add_u32 s58, s28, 0x10000\n"
  "s_add_u32 s59, s28, 0x20000\n"
  "s_add_u32 s60, s28, 0x30000\n"
  "s_cmp_ge_u32 s79, 3\n"
  "s_cselect_b32 s29, s58, 2146435072\n"
  "s_cselect_b32 s30, s59, s58\n"
  "s_cmp_ge_u32 s79, 2\n"
  "s_cselect_b32 s30, s30, 2146435072\n"
  "s_cmp_ge_u32 s79, 4\n"
  "s_cselect_b32 s31, s60, 2146435072\n"
  "s_add_u32 m0, s82, 0\n"
  "s_nop 0\n"
  "buffer_load_dwordx4 v222, s[16:19], s28 offen offset:0 lds\n"
  "buffer_load_dwordx4 v223, s[16:19], s28 offen offset:1024 lds\n"
  "buffer_load_dwordx4 v224, s[16:19], s28 offen offset:2048 lds\n"
  "buffer_load_dwordx4 v225, s[16:19], s28 offen offset:3072 lds\n"
  "s_add_u32 m0, s82, 4096\n"
  "s_nop 0\n"
  "buffer_load_dwordx4 v222, s[16:19], s29 offen offset:0 lds\n"
  "s_add_u32 s39, s39, s62\n"
  "s_sub_u32 s47, s47, s64\n"
  "s_mov_b32 s14, s10\n"
  "s_add_u32 s46, s45, 65536\n"
  "s_mov_b32 s43, 32768\n"
  "s_mov_b32 s42, 32768\n"
  "buffer_load_dwordx4 v223, s[16:19], s29 offen offset:1024 lds\n"
  "s_mov_b32 s38, 0x3e0293ee\n"
  "s_mov_b32 s54, 0\n"
  "s_mov_b32 s51, 2146435072\n"
  "s_mov_b32 s70, 0\n"
  "s_lshl_b32 s81, s53, 14\n"
  "v_mov_b32 v236, 0\n"
  "buffer_load_dwordx4 v224, s[16:19], s29 offen offset:2048 lds\n"
  "v_mov_b32 v237, 0\n"
  "v_mov_b32 v212, 0x3f803f80\n"
  "v_mov_b32 v213, 0x3f803f80\n"
  "v_mov_b32 v214, 0x3f803f80\n"
  "v_mov_b32 v215, 0x3f803f80\n"
  "v_mov_b32 v216, 0x3f803f80\n"
  "buffer_load_dwordx4 v225, s[16:19], s29 offen offset:3072 lds\n"
  "v_mov_b32 v217, 0x3f803f80\n"
  "v_mov_b32 v218, 0x3f803f80\n"
  "v_mov_b32 v219, 0x3f803f80\n"
  "v_mov_b32 v232, s56\n"
  "v_mov_b32 v233, s57\n"
  "v_mfma_f32_32x32x8_bf16 v[0:15], v[236:237], v[236:237], 0\n"
  "s_add_u32 m0, s82, 8192\n"
  "s_nop 0\n"
  "buffer_load_dwordx4 v222, s[16:19], s30 offen offset:0 lds\n"
  "s_add_u32 s58, s70, 6\n"
  "s_add_u32 s59, s70, 7\n"
  "s_add_u32 s60, s70, 3\n"
  "s_add_u32 s61, s70, 5\n"
  "v_mfma_f32_32x32x8_bf16 v[16:31], v[236:237], v[236:237], 0\n"
  "v_readlane_b32 s77, v228, s58\n"
  "buffer_load_dwordx4 v223, s[16:19], s30 offen offset:1024 lds\n"
  "v_readlane_b32 s73, v228, s59\n"
  "v_readlane_b32 s48, v228, s60\n"
  "v_readlane_b32 s72, v228, s61\n"
  "v_mfma_f32_32x32x8_bf16 v[32:47], v[236:237], v[236:237], 0\n"
  "s_mov_b32 s22, s18\n"
  "s_mov_b64 s[34:35], s[28:29]\n"
  "buffer_load_dwordx4 v224, s[16:19], s30 offen offset:2048 lds\n"
  "s_mov_b64 s[36:37], s[30:31]\n"
  "s_mov_b32 s78, s79\n"
  "v_mfma_f32_32x32x8_bf16 v[48:63], v[236:237], v[236:237], 0\n"
  "s_and_b32 s74, s73, 0xff\n"
  "s_andn2_b32 s73, s73, 0xff\n"
  "s_mov_b32 s75, 0\n"
  "buffer_load_dwordx4 v225, s[16:19], s30 offen offset:3072 lds\n"
  "s_max_u32 s87, s74, 1\n"
  "v_mfma_f32_32x32x8_bf16 v[64:79], v[236:237], v[236:237], 0\n"
  "s_sub_u32 s87, s87, 1\n"
  "s_cmp_eq_u32 s78, 4\n"
  "s_cselect_b32 s76, 0, 2\n"
  "s_cmp_eq_u32 s74, 0\n"
  "v_mfma_f32_32x32x8_bf16 v[80:95], v[236:237], v[236:237], 0\n"
  "s_cselect_b32 s76, 2, s76\n"
  "s_add_u32 m0, s82, 12288\n"
  "s_nop 0\n"
  "buffer_load_dwordx4 v222, s[16:19], s31 offen offset:0 lds\n"
  "s_cmp_gt_u32 s74, 2\n"
  "s_cselect_b32 s76, 2, s76\n"
  "s_add_u32 s58, s73, 69632\n"
  "v_mfma_f32_32x32x8_bf16 v[96:111], v[236:237], v[236:237], 0\n"
  "s_cmp_lg_u32 s74, 0\n"
  "s_cselect_b32 s50, s58, 2146435072\n"
  "buffer_load_dwordx4 v223, s[16:19], s31 offen offset:1024 lds\n"
  "s_lshr_b32 s80, s70, 3\n"
  "s_and_b32 s48, s48, 0xffff\n"
  "v_mfma_f32_32x32x8_bf16 v[112:127], v[236:237], v[236:237], 0\n"
  "s_sub_u32 s48, s48, 1\n"
  "s_mov_b32 s83, s48\n"
  "v_mov_b32 v230, 0\n"
  "buffer_load_dwordx4 v224, s[16:19], s31 offen offset:2048 lds\n"
  "v_mov_b32 v231, 0\n"
  "v_mfma_f32_32x32x8_bf16 v[192:207], v[236:237], v[236:237], 0\n"
  "buffer_load_dwordx4 v225, s[16:19], s31 offen offset:3072 lds\n"
  "s_cmp_lg_u32 s47, 0\n"
  "s_cbranch_scc1 L_sw_doneP0\n"
  "s_add_u32 s71, s71, 8\n"
  "s_add_u32 s58, s71, 1\n"
  "s_add_u32 s59, s71, 2\n"
  "s_add_u32 s60, s71, 3\n"
  "v_readlane_b32 s10, v228, s58\n"
  "v_readlane_b32 s39, v228, s59\n"
  "v_readlane_b32 s47, v228, s60\n"
  "s_add_u32 s58, s71, 8\n"
  "s_and_b32 s47, s47, 0xffff\n"
  "s_lshl_b32 s59, s47, 18\n"
  "s_add_u32 s59, s59, s39\n"
  "s_add_u32 s59, s59, 256\n"
  "s_min_u32 s10, s10, s59\n"
  "s_add_u32 s39, s39, s52\n"
  "s_cmp_ge_u32 s58, s55\n"
  "s_cselect_b32 s47, 0x10000, s47\n"
  "L_sw_doneP0:\n"
  "s_sub_u32 s47, s47, 1\n"
  "s_or_b32 m0, s45, s43\n"
  "s_nop 0\n"
  "buffer_load_dwordx4 v222, s[8:11], s39 offen offset:0 lds\n"
  "buffer_load_dwordx4 v223, s[8:11], s39 offen offset:1024 lds\n"
  "buffer_load_dwordx4 v224, s[8:11], s39 offen offset:2048 lds\n"
  "buffer_load_dwordx4 v225, s[8:11], s39 offen offset:3072 lds\n"
  "s_cmp_lt_u32 s72, 32\n"
  "s_cselect_b32 s60, 1, 0\n"
  "s_sub_u32 s49, s48, s60\n"
  "s_cbranch_scc0 L_P_nm\n"
  "v_lshrrev_b32 v235, 8, v226\n"
  "s_mul_i32 s58, s72, 15\n"
  "s_mov_b32 s59, 0x3f803f80\n"
  "v_mul_u32_u24 v235, 60, v235\n"
  "v_sub_u32 v235, s58, v235\n"
  "v_subrev_u32 v212, 0, v235\n"
  "v_subrev_u32 v213, 30, v235\n"
  "v_subrev_u32 v214, 240, v235\n"
  "v_subrev_u32 v215, 270, v235\n"
  "v_med3_i32 v212, v212, 0, 30\n"
  "v_med3_i32 v213, v213, 0, 30\n"
  "v_med3_i32 v214, v214, 0, 30\n"
  "v_med3_i32 v215, v215, 0, 30\n"
  "v_bfe_u32 v212, s59, 0, v212\n"
  "v_bfe_u32 v213, s59, 0, v213\n"
  "v_bfe_u32 v214, s59, 0, v214\n"
  "v_bfe_u32 v215, s59, 0, v215\n"
  "s_mov_b32 s49, 0\n"
  "L_P_nm:\n"
  "s_sub_u32 s58, s82, s42\n"
  "v_add_u32 v234, s58, v220\n"
  "v_xor_b32 v235, 49152, v220\n"
  "s_waitcnt vmcnt(12)\n"
  "s_barrier\n"
  "ds_read_b128 a[224:227], v235 offset:0\n"
  "ds_read_b128 a[228:231], v235 offset:512\n"
  "ds_read_b128 a[232:235], v235 offset:1024\n"
  "ds_read_b128 a[236:239], v235 offset:1536\n"
  "ds_read_b128 a[0:3], v234 offset:0\n"
  "ds_read_b128 a[4:7], v234 offset:512\n"
  "ds_read_b128 a[8:11], v234 offset:1024\n"
  "ds_read_b128 a[12:15], v234 offset:1536\n"
  "ds_read_b128 a[16:19], v234 offset:4096\n"
  "ds_read_b128 a[20:23], v234 offset:4608\n"
  "ds_read_b128 a[24:27], v234 offset:5120\n"
  "ds_read_b128 a[28:31], v234 offset:5632\n"
  "ds_read_b128 a[64:67], v235 offset:8192\n"
  "ds_read_b128 a[68:71], v235 offset:8704\n"
  "ds_read_b128 a[72:75], v235 offset:9216\n"
  "ds_read_b128 a[76:79], v235 offset:9728\n"
  "ds_read_b128 a[80:83], v235 offset:12288\n"
  "ds_read_b128 a[84:87], v235 offset:12800\n"
  "ds_read_b128 a[88:91], v235 offset:13312\n"
  "ds_read_b128 a[92:95], v235 offset:13824\n"
  "s_or_b32 m0, s46, s43\n"
  "s_nop 0\n"
  "buffer_load_dwordx4 v222, s[12:15], s41 offen offset:0 lds\n"
  "buffer_load_dwordx4 v223, s[12:15], s41 offen offset:1024 lds\n"
  "buffer_load_dwordx4 v224, s[12:15], s41 offen offset:2048 lds\n"
  "buffer_load_dwordx4 v225, s[12:15], s41 offen offset:3072 lds\n"
  "s_mov_b32 s41, s39\n"
  "s_mov_b32 s14, s10\n"
  "s_add_u32 s39, s39, 0x40000\n"
  "s_mov_b32 s43, 49152\n"
  "s_cmp_lg_u32 s47, 0\n"
  "s_cbranch_scc1 L_sw_doneP1\n"
  "s_add_u32 s71, s71, 8\n"
  "s_add_u32 s58, s71, 1\n"
  "s_add_u32 s59, s71, 2\n"
  "s_add_u32 s60, s71, 3\n"
  "v_readlane_b32 s10, v228, s58\n"
  "v_readlane_b32 s39, v228, s59\n"
  "v_readlane_b32 s47, v228, s60\n"
  "s_add_u32 s58, s71, 8\n"
  "s_and_b32 s47, s47, 0xffff\n"
  "s_lshl_b32 s59, s47, 18\n"
  "s_add_u32 s59, s59, s39\n"
  "s_add_u32 s59, s59, 256\n"
  "s_min_u32 s10, s10, s59\n"
  "s_add_u32 s39, s39, s52\n"
  "s_cmp_ge_u32 s58, s55\n"
  "s_cselect_b32 s47, 0x10000, s47\n"
  "L_sw_doneP1:\n"
  "s_sub_u32 s47, s47, 1\n"
  "s_waitcnt lgkmcnt(8)\n"
  "s_or_b32 m0, s45, s43\n"
  "v_mfma_f32_16x16x32_bf16 v[240:243], a[224:227], a[0:3], 0\n"
  "buffer_load_dwordx4 v222, s[8:11], s39 offen offset:0 lds\n"
  "v_mfma_f32_16x16x32_bf16 v[240:243], a[228:231], a[4:7], v[240:243]\n"
  "v_mfma_f32_16x16x32_bf16 v[240:243], a[232:235], a[8:11], v[240:243]\n"
  "buffer_load_dwordx4 v223, s[8:11], s39 offen offset:1024 lds\n"
  "v_mfma_f32_16x16x32_bf16 v[240:243], a[236:239], a[12:15], v[240:243]\n"
  "v_mfma_f32_16x16x32_bf16 v[244:247], a[224:227], a[16:19], 0\n"
  "buffer_load_dwordx4 v224, s[8:11], s39 offen offset:2048 lds\n"
  "v_mfma_f32_16x16x32_bf16 v[244:247], a[228:231], a[20:23], v[244:247]\n"
  "v_mfma_f32_16x16x32_bf16 v[244:247], a[232:235], a[24:27], v[244:247]\n"
  "buffer_load_dwordx4 v225, s[8:11], s39 offen offset:3072 lds\n"
  "v_mfma_f32_16x16x32_bf16 v[244:247], a[236:239], a[28:31], v[244:247]\n"
  "s_waitcnt vmcnt(12) lgkmcnt(0)\n"
  "v_mfma_f32_16x16x32_bf16 v[128:131], a[64:67], a[0:3], 0\n"
  "ds_read_b128 a[32:35], v234 offset:8192\n"
  "v_max3_f32 v240, v240, v241, v242\n"
  "v_mfma_f32_16x16x32_bf16 v[128:131], a[68:71], a[4:7], v[128:131]\n"
  "ds_read_b128 a[36:39], v234 offset:8704\n"
  "s_nop 1\n"
  "v_max3_f32 v244, v244, v245, v246\n"
  "v_mfma_f32_16x16x32_bf16 v[128:131], a[72:75], a[8:11], v[128:131]\n"
  "ds_read_b128 a[40:43], v234 offset:9216\n"
  "v_max_f32 v240, v240, v243\n"
  "v_mfma_f32_16x16x32_bf16 v[128:131], a[76:79], a[12:15], v[128:131]\n"
  "ds_read_b128 a[44:47], v234 offset:9728\n"
  "v_max_f32 v244, v244, v247\n"
  "v_mfma_f32_16x16x32_bf16 v[132:135], a[80:83], a[0:3], 0\n"
  "ds_read_b128 a[48:51], v234 offset:12288\n"
  "v_mov_b32 v241, v240\n"
  "v_mfma_f32_16x16x32_bf16 v[132:135], a[84:87], a[4:7], v[132:135]\n"
  "ds_read_b128 a[52:55], v234 offset:12800\n"
  "v_mov_b32 v245, v244\n"
  "v_mfma_f32_16x16x32_bf16 v[132:135], a[88:91], a[8:11], v[132:135]\n"
  "ds_read_b128 a[56:59], v234 offset:13312\n"
  "v_mfma_f32_16x16x32_bf16 v[132:135], a[92:95], a[12:15], v[132:135]\n"
  "ds_read_b128 a[60:63], v234 offset:13824\n"
  "s_or_b32 m0, s46, s43\n"
  "v_mfma_f32_16x16x32_bf16 v[136:139], a[64:67], a[16:19], 0\n"
  "buffer_load_dwordx4 v222, s[12:15], s41 offen offset:0 lds\n"
  "v_mfma_f32_16x16x32_bf16 v[136:139], a[68:71], a[20:23], v[136:139]\n"
  "v_permlane32_swap_b32 v241, v240\n"
  "v_mfma_f32_16x16x32_bf16 v[136:139], a[72:75], a[24:27], v[136:139]\n"
  "buffer_load_dwordx4 v223, s[12:15], s41 offen offset:1024 lds\n"
  "v_mfma_f32_16x16x32_bf16 v[136:139], a[76:79], a[28:31], v[136:139]\n"
  "v_permlane32_swap_b32 v245, v244\n"
  "v_mfma_f32_16x16x32_bf16 v[140:143], a[80:83], a[16:19], 0\n"
  "buffer_load_dwordx4 v224, s[12:15], s41 offen offset:2048 lds\n"
  "v_max_f32 v240, v240, v241\n"
  "v_mfma_f32_16x16x32_bf16 v[140:143], a[84:87], a[20:23], v[140:143]\n"
  "v_max_f32 v244, v244, v245\n"
  "v_mov_b32 v241, v240\n"
  "v_mfma_f32_16x16x32_bf16 v[140:143], a[88:91], a[24:27], v[140:143]\n"
  "buffer_load_dwordx4 v225, s[12:15], s41 offen offset:3072 lds\n"
  "v_mov_b32 v245, v244\n"
  "v_mfma_f32_16x16x32_bf16 v[140:143], a[92:95], a[28:31], v[140:143]\n"
  "v_permlane16_swap_b32 v241, v240\n"
  "v_permlane16_swap_b32 v245, v244\n"
  "v_max_f32 v240, v240, v241\n"
  "v_max_f32 v244, v244, v245\n"
  "v_mul_f32 v208, -v240, s38\n"
  "v_mul_f32 v209, -v244, s38\n"
  "s_mov_b32 s41, s39\n"
  "s_mov_b32 s14, s10\n"
  "s_add_u32 s39, s39, 0x40000\n"
  "s_mov_b32 s43, 16384\n"
  "s_waitcnt lgkmcnt(0)\n"
  "v_mfma_f32_16x16x32_bf16 v[248:251], a[224:227], a[32:35], 0\n"
  "v_fma_f32 v128, v128, s38, v208\n"
  "v_fma_f32 v129, v129, s38, v208\n"
  "v_mfma_f32_16x16x32_bf16 v[248:251], a[228:231], a[36:39], v[248:251]\n"
  "v_exp_f32 v128, v128\n"
  "v_fma_f32 v130, v130, s38, v208\n"
  "v_mfma_f32_16x16x32_bf16 v[248:251], a[232:235], a[40:43], v[248:251]\n"
  "v_exp_f32 v129, v129\n"
  "v_mfma_f32_16x16x32_bf16 v[248:251], a[236:239], a[44:47], v[248:251]\n"
  "v_cvt_pk_bf16_f32 v128, v128, v129\n"
  "v_fma_f32 v131, v131, s38, v208\n"
  "v_mfma_f32_16x16x32_bf16 v[252:255], a[224:227], a[48:51], 0\n"
  "v_exp_f32 v130, v130\n"
  "v_fma_f32 v132, v132, s38, v208\n"
  "v_mfma_f32_16x16x32_bf16 v[252:255], a[228:231], a[52:55], v[252:255]\n"
  "v_exp_f32 v131, v131\n"
  "v_mfma_f32_16x16x32_bf16 v[252:255], a[232:235], a[56:59], v[252:255]\n"
  "v_cvt_pk_bf16_f32 v129, v130, v131\n"
  "v_fma_f32 v133, v133, s38, v208\n"
  "v_mfma_f32_16x16x32_bf16 v[252:255], a[236:239], a[60:63], v[252:255]\n"
  "v_exp_f32 v132, v132\n"
  "v_fma_f32 v134, v134, s38, v208\n"
  "s_waitcnt vmcnt(8)\n"
  "s_barrier\n"
  "ds_read_b128 a[192:195], v220 offset:0\n"
  "ds_read_b128 a[196:199], v220 offset:512\n"
  "ds_read_b128 a[200:203], v220 offset:1024\n"
  "ds_read_b128 a[204:207], v220 offset:1536\n"
  "ds_read_b128 a[208:211], v220 offset:4096\n"
  "ds_read_b128 a[212:215], v220 offset:4608\n"
  "ds_read_b128 a[216:219], v220 offset:5120\n"
  "ds_read_b128 a[220:223], v220 offset:5632\n"
  "v_mfma_f32_16x16x32_bf16 v[144:147], a[64:67], a[32:35], 0\n"
  "v_exp_f32 v133, v133\n"
  "v_mfma_f32_16x16x32_bf16 v[144:147], a[68:71], a[36:39], v[144:147]\n"
  "v_cvt_pk_bf16_f32 v130, v132, v133\n"
  "v_fma_f32 v135, v135, s38, v208\n"
  "v_mfma_f32_16x16x32_bf16 v[144:147], a[72:75], a[40:43], v[144:147]\n"
  "v_exp_f32 v134, v134\n"
  "v_fma_f32 v136, v136, s38, v209\n"
  "v_mfma_f32_16x16x32_bf16 v[144:147], a[76:79], a[44:47], v[144:147]\n"
  "v_exp_f32 v135, v135\n"
  "v_mfma_f32_16x16x32_bf16 v[148:151], a[80:83], a[32:35], 0\n"
  "v_cvt_pk_bf16_f32 v131, v134, v135\n"
  "v_fma_f32 v137, v137, s38, v209\n"
  "v_mfma_f32_16x16x32_bf16 v[148:151], a[84:87], a[36:39], v[148:151]\n"
  "v_exp_f32 v136, v136\n"
  "v_fma_f32 v138, v138, s38, v209\n"
  "v_mfma_f32_16x16x32_bf16 v[148:151], a[88:91], a[40:43], v[148:151]\n"
  "v_exp_f32 v137, v137\n"
  "v_mfma_f32_16x16x32_bf16 v[148:151], a[92:95], a[44:47], v[148:151]\n"
  "v_cvt_pk_bf16_f32 v136, v136, v137\n"
  "v_fma_f32 v139, v139, s38, v209\n"
  "v_mfma_f32_16x16x32_bf16 v[152:155], a[64:67], a[48:51], 0\n"
  "v_exp_f32 v138, v138\n"
  "v_fma_f32 v140, v140, s38, v209\n"
  "v_mfma_f32_16x16x32_bf16 v[152:155], a[68:71], a[52:55], v[152:155]\n"
  "v_exp_f32 v139, v139\n"
  "v_mfma_f32_16x16x32_bf16 v[152:155], a[72:75], a[56:59], v[152:155]\n"
  "v_cvt_pk_bf16_f32 v137, v138, v139\n"
  "v_fma_f32 v141, v141, s38, v209\n"
  "v_mfma_f32_16x16x32_bf16 v[152:155], a[76:79], a[60:63], v[152:155]\n"
  "v_exp_f32 v140, v140\n"
  "v_fma_f32 v142, v142, s38, v209\n"
  "v_mfma_f32_16x16x32_bf16 v[156:159], a[80:83], a[48:51], 0\n"
  "v_exp_f32 v141, v141\n"
  "v_mfma_f32_16x16x32_bf16 v[156:159], a[84:87], a[52:55], v[156:159]\n"
  "v_cvt_pk_bf16_f32 v138, v140, v141\n"
  "v_fma_f32 v143, v143, s38, v209\n"
  "v_mfma_f32_16x16x32_bf16 v[156:159], a[88:91], a[56:59], v[156:159]\n"
  "v_exp_f32 v142, v142\n"
  "v_mfma_f32_16x16x32_bf16 v[156:159], a[92:95], a[60:63], v[156:159]\n"
  "v_exp_f32 v143, v143\n"
  "s_nop 0\n"
  "v_cvt_pk_bf16_f32 v139, v142, v143\n"
  "s_cmp_lg_u32 s47, 0\n"
  "s_cbranch_scc1 L_sw_doneP2\n"
  "s_add_u32 s71, s71, 8\n"
  "s_add_u32 s58, s71, 1\n"
  "s_add_u32 s59, s71, 2\n"
  "s_add_u32 s60, s71, 3\n"
  "v_readlane_b32 s10, v228, s58\n"
  "v_readlane_b32 s39, v228, s59\n"
  "v_readlane_b32 s47, v228, s60\n"
  "s_add_u32 s58, s71, 8\n"
  "s_and_b32 s47, s47, 0xffff\n"
  "s_lshl_b32 s59, s47, 18\n"
  "s_add_u32 s59, s59, s39\n"
  "s_add_u32 s59, s59, 256\n"
  "s_min_u32 s10, s10, s59\n"
  "s_add_u32 s39, s39, s52\n"
  "s_cmp_ge_u32 s58, s55\n"
  "s_cselect_b32 s47, 0x10000, s47\n"
  "L_sw_doneP2:\n"
  "s_sub_u32 s47, s47, 1\n"
  "s_min_u32 s58, s49, s47\n"
  "s_sub_i32 s59, s48, 3\n"
  "s_max_i32 s59, s59, 0\n"
  "s_cmp_eq_u32 s76, 0\n"
  "s_cselect_b32 s59, s59, s58\n"
  "s_min_u32 s58, s58, s59\n"
  "s_sub_u32 s48, s48, s58\n"
  "s_sub_u32 s49, s49, s58\n"
  "s_sub_u32 s47, s47, s58\n"
  "s_sub_u32 s44, s58, 0\n"
  "s_add_u32 s62, s70, 8\n"
  "s_cmp_ge_u32 s62, s55\n"
  "s_cbranch_scc1 L_nopfP\n"
  "s_add_u32 s58, s62, 0\n"
  "s_add_u32 s59, s62, 1\n"
  "s_add_u32 s61, s62, 3\n"
  "v_readlane_b32 s79, v228, s61\n"
  "v_readlane_b32 s28, v228, s58\n"
  "v_readlane_b32 s18, v228, s59\n"
  "s_lshr_b32 s79, s79, 16\n"
  "s_mul_i32 s61, s53, s79\n"
  "s_lshl_b32 s61, s61, 16\n"
  "s_add_u32 s28, s28, s61\n"
  "s_add_u32 s58, s28, 0x10000\n"
  "s_add_u32 s59, s28, 0x20000\n"
  "s_add_u32 s60, s28, 0x30000\n"
  "s_cmp_ge_u32 s79, 3\n"
  "s_cselect_b32 s29, s58, 2146435072\n"
  "s_cselect_b32 s30, s59, s58\n"
  "s_cmp_ge_u32 s79, 2\n"
  "s_cselect_b32 s30, s30, 2146435072\n"
  "s_cmp_ge_u32 s79, 4\n"
  "s_cselect_b32 s31, s60, 2146435072\n"
  "s_cmp_lg_u32 s44, 0\n"
  "s_cselect_b32 s58, s78, 0\n"
  "s_cmp_eq_u32 s58, 4\n"
  "s_cselect_b32 s78, 5, s78\n"
  "s_cbranch_scc1 L_nopfP\n"
  "s_add_u32 m0, s82, 0\n"
  "s_nop 0\n"
  "buffer_load_dwordx4 v222, s[16:19], s28 offen offset:0 lds\n"
  "buffer_load_dwordx4 v223, s[16:19], s28 offen offset:1024 lds\n"
  "buffer_load_dwordx4 v224, s[16:19], s28 offen offset:2048 lds\n"
  "buffer_load_dwordx4 v225, s[16:19], s28 offen offset:3072 lds\n"
  "s_add_u32 m0, s82, 4096\n"
  "s_nop 0\n"
  "buffer_load_dwordx4 v222, s[16:19], s29 offen offset:0 lds\n"
  "buffer_load_dwordx4 v223, s[16:19], s29 offen offset:1024 lds\n"
  "buffer_load_dwordx4 v224, s[16:19], s29 offen offset:2048 lds\n"
  "buffer_load_dwordx4 v225, s[16:19], s29 offen offset:3072 lds\n"
  "s_add_u32 m0, s82, 8192\n"
  "s_nop 0\n"
  "buffer_load_dwordx4 v222, s[16:19], s30 offen offset:0 lds\n"
  "buffer_load_dwordx4 v223, s[16:19], s30 offen offset:1024 lds\n"
  "buffer_load_dwordx4 v224, s[16:19], s30 offen offset:2048 lds\n"
  "buffer_load_dwordx4 v225, s[16:19], s30 offen offset:3072 lds\n"
  "s_add_u32 m0, s82, 12288\n"
  "s_nop 0\n"
  "buffer_load_dwordx4 v222, s[16:19], s31 offen offset:0 lds\n"
  "buffer_load_dwordx4 v223, s[16:19], s31 offen offset:1024 lds\n"
  "buffer_load_dwordx4 v224, s[16:19], s31 offen offset:2048 lds\n"
  "buffer_load_dwordx4 v225, s[16:19], s31 offen offset:3072 lds\n"
  "L_nopfP:\n"
  "s_cmp_ge_u32 s78, 3\n"
  "s_cbranch_scc1 L_hrP\n"
  "s_or_b32 m0, s45, s43\n"
  "s_nop 0\n"
  "buffer_load_dwordx4 v222, s[8:11], s39 offen offset:0 lds\n"
  "buffer_load_dwordx4 v223, s[8:11], s39 offen offset:1024 lds\n"
  "buffer_load_dwordx4 v224, s[8:11], s39 offen offset:2048 lds\n"
  "buffer_load_dwordx4 v225, s[8:11], s39 offen offset:3072 lds\n"
  "s_or_b32 m0, s46, s43\n"
  "s_nop 0\n"
  "buffer_load_dwordx4 v222, s[12:15], s41 offen offset:0 lds\n"
  "buffer_load_dwordx4 v223, s[12:15], s41 offen offset:1024 lds\n"
  "buffer_load_dwordx4 v224, s[12:15], s41 offen offset:2048 lds\n"
  "buffer_load_dwordx4 v225, s[12:15], s41 offen offset:3072 lds\n"
  "L_hrP:\n"
  "v_max3_f32 v248, v248, v249, v250\n"
  "v_max3_f32 v252, v252, v253, v254\n"
  "v_max_f32 v248, v248, v251\n"
  "v_max_f32 v252, v252, v255\n"
  "v_mov_b32 v249, v248\n"
  "v_mov_b32 v253, v252\n"
  "s_nop 0\n"
  "v_permlane32_swap_b32 v249, v248\n"
  "v_permlane32_swap_b32 v253, v252\n"
  "v_max_f32 v248, v248, v249\n"
  "v_max_f32 v252, v252, v253\n"
  "v_mov_b32 v249, v248\n"
  "v_mov_b32 v253, v252\n"
  "s_nop 0\n"
  "v_permlane16_swap_b32 v249, v248\n"
  "v_permlane16_swap_b32 v253, v252\n"
  "v_max_f32 v248, v248, v249\n"
  "v_max_f32 v252, v252, v253\n"
  "v_mul_f32 v210, -v248, s38\n"
  "v_mul_f32 v211, -v252, s38\n"
  "s_waitcnt lgkmcnt(0)\n"
  "s_cmp_eq_u32 s78, 5\n"
  "s_cselect_b32 s78, 4, s78\n"
  "s_cbranch_scc1 L_loop4q_G2\n"
  "s_cmp_eq_u32 s78, 4\n"
  "s_cbranch_scc1 L_loop4_G2\n"
  "s_cmp_eq_u32 s78, 3\n"
  "s_cbranch_scc1 L_loop3_G2\n"
  "s_cmp_eq_u32 s78, 2\n"
  "s_cbranch_scc1 L_loop2_G2\n"
  "s_branch L_loop1_G2\n"
  "L_loop4q_G2:\n"
  "v_mfma_f32_16x16x32_bf16 v[160:163], a[192:195], a[0:3], 0\n"
  "ds_read_b64_tr_b16 a[224:225], v221 offset:8192\n"
  "v_fma_f32 v144, v144, s38, v210\n"
  "v_fma_f32 v145, v145, s38, v210\n"
  "v_mfma_f32_16x16x32_bf16 v[160:163], a[196:199], a[4:7], v[160:163]\n"
  "v_exp_f32 v144, v144\n"
  "v_mfma_f32_16x16x32_bf16 v[160:163], a[200:203], a[8:11], v[160:163]\n"
  "ds_read_b64_tr_b16 a[226:227], v221 offset:12288\n"
  "v_exp_f32 v145, v145\n"
  "v_mfma_f32_16x16x32_bf16 v[160:163], a[204:207], a[12:15], v[160:163]\n"
  "ds_read_b64_tr_b16 a[228:229], v239 offset:8192\n"
  "v_cvt_pk_bf16_f32 v144, v144, v145\n"
  "v_fma_f32 v146, v146, s38, v210\n"
  "v_mfma_f32_16x16x32_bf16 v[164:167], a[208:211], a[0:3], 0\n"
  "v_exp_f32 v146, v146\n"
  "v_mfma_f32_16x16x32_bf16 v[164:167], a[212:215], a[4:7], v[164:167]\n"
  "s_or_b32 m0, s45, s43\n"
  "v_fma_f32 v147, v147, s38, v210\n"
  "v_fma_f32 v148, v148, s38, v210\n"
  "v_mfma_f32_16x16x32_bf16 v[164:167], a[216:219], a[8:11], v[164:167]\n"
  "buffer_load_dwordx4 v222, s[8:11], s39 offen offset:0 lds\n"
  "v_fma_f32 v149, v149, s38, v210\n"
  "v_mfma_f32_16x16x32_bf16 v[164:167], a[220:223], a[12:15], v[164:167]\n"
  "v_exp_f32 v147, v147\n"
  "v_mfma_f32_16x16x32_bf16 v[168:171], a[192:195], a[16:19], 0\n"
  "ds_read_b64_tr_b16 a[230:231], v239 offset:12288\n"
  "v_cvt_pk_bf16_f32 v145, v146, v147\n"
  "v_fma_f32 v150, v150, s38, v210\n"
  "v_mfma_f32_16x16x32_bf16 v[168:171], a[196:199], a[20:23], v[168:171]\n"
  "ds_read_b64_tr_b16 a[232:233], v221 offset:8704\n"
  "v_exp_f32 v148, v148\n"
  "v_mfma_f32_16x16x32_bf16 v[168:171], a[200:203], a[24:27], v[168:171]\n"
  "ds_read_b64_tr_b16 a[234:235], v221 offset:12800\n"
  "v_exp_f32 v149, v149\n"
  "v_mfma_f32_16x16x32_bf16 v[168:171], a[204:207], a[28:31], v[168:171]\n"
  "v_cvt_pk_bf16_f32 v146, v148, v149\n"
  "v_fma_f32 v151, v151, s38, v210\n"
  "v_mfma_f32_16x16x32_bf16 v[172:175], a[208:211], a[16:19], 0\n"
  "buffer_load_dwordx4 v223, s[8:11], s39 offen offset:1024 lds\n"
  "v_fma_f32 v152, v152, s38, v211\n"
  "v_mfma_f32_16x16x32_bf16 v[172:175], a[212:215], a[20:23], v[172:175]\n"
  "ds_read_b64_tr_b16 a[236:237], v239 offset:8704\n"
  "v_exp_f32 v150, v150\n"
  "v_mfma_f32_16x16x32_bf16 v[172:175], a[216:219], a[24:27], v[172:175]\n"
  "v_exp_f32 v151, v151\n"
  "v_mfma_f32_16x16x32_bf16 v[172:175], a[220:223], a[28:31], v[172:175]\n"
  "ds_read_b64_tr_b16 a[238:239], v239 offset:12800\n"
  "v_cvt_pk_bf16_f32 v147, v150, v151\n"
  "v_fma_f32 v153, v153, s38, v211\n"
  "v_mfma_f32_16x16x32_bf16 v[176:179], a[192:195], a[32:35], 0\n"
  "ds_read_b64_tr_b16 a[240:241], v221 offset:9216\n"
  "v_exp_f32 v152, v152\n"
  "v_mfma_f32_16x16x32_bf16 v[176:179], a[196:199], a[36:39], v[176:179]\n"
  "buffer_load_dwordx4 v224, s[8:11], s39 offen offset:2048 lds\n"
  "v_mfma_f32_16x16x32_bf16 v[176:179], a[200:203], a[40:43], v[176:179]\n"
  "ds_read_b64_tr_b16 a[242:243], v221 offset:13312\n"
  "v_exp_f32 v153, v153\n"
  "v_mfma_f32_16x16x32_bf16 v[176:179], a[204:207], a[44:47], v[176:179]\n"
  "ds_read_b64_tr_b16 a[244:245], v239 offset:9216\n"
  "v_cvt_pk_bf16_f32 v152, v152, v153\n"
  "v_fma_f32 v154, v154, s38, v211\n"
  "v_mfma_f32_16x16x32_bf16 v[180:183], a[208:211], a[32:35], 0\n"
  "v_exp_f32 v154, v154\n"
  "v_mfma_f32_16x16x32_bf16 v[180:183], a[212:215], a[36:39], v[180:183]\n"
  "ds_read_b64_tr_b16 a[246:247], v239 offset:13312\n"
  "ds_read_b64_tr_b16 a[248:249], v221 offset:9728\n"
  "v_fma_f32 v155, v155, s38, v211\n"
  "v_mfma_f32_16x16x32_bf16 v[180:183], a[216:219], a[40:43], v[180:183]\n"
  "ds_read_b64_tr_b16 a[250:251], v221 offset:13824\n"
  "v_exp_f32 v155, v155\n"
  "v_mfma_f32_16x16x32_bf16 v[180:183], a[220:223], a[44:47], v[180:183]\n"
  "buffer_load_dwordx4 v225, s[8:11], s39 offen offset:3072 lds\n"
  "v_mfma_f32_16x16x32_bf16 v[184:187], a[192:195], a[48:51], 0\n"
  "ds_read_b64_tr_b16 a[252:253], v239 offset:9728\n"
  "ds_read_b64_tr_b16 a[254:255], v239 offset:13824\n"
  "v_cvt_pk_bf16_f32 v153, v154, v155\n"
  "v_mfma_f32_16x16x32_bf16 v[184:187], a[196:199], a[52:55], v[184:187]\n"
  "v_xor_b32 v221, s43, v221\n"
  "v_xor_b32 v239, s43, v239\n"
  "v_fma_f32 v156, v156, s38, v211\n"
  "v_mfma_f32_16x16x32_bf16 v[184:187], a[200:203], a[56:59], v[184:187]\n"
  "v_exp_f32 v156, v156\n"
  "v_fma_f32 v157, v157, s38, v211\n"
  "v_mfma_f32_16x16x32_bf16 v[184:187], a[204:207], a[60:63], v[184:187]\n"
  "v_exp_f32 v157, v157\n"
  "v_mfma_f32_16x16x32_bf16 v[188:191], a[208:211], a[48:51], 0\n"
  "s_or_b32 m0, s46, s43\n"
  "v_cvt_pk_bf16_f32 v154, v156, v157\n"
  "v_fma_f32 v158, v158, s38, v211\n"
  "v_mfma_f32_16x16x32_bf16 v[188:191], a[212:215], a[52:55], v[188:191]\n"
  "buffer_load_dwordx4 v222, s[12:15], s41 offen offset:0 lds\n"
  "v_fma_f32 v159, v159, s38, v211\n"
  "v_mfma_f32_16x16x32_bf16 v[188:191], a[216:219], a[56:59], v[188:191]\n"
  "v_exp_f32 v158, v158\n"
  "v_mfma_f32_16x16x32_bf16 v[188:191], a[220:223], a[60:63], v[188:191]\n"
  "v_exp_f32 v159, v159\n"
  "s_waitcnt lgkmcnt(0)\n"
  "v_cvt_pk_bf16_f32 v155, v158, v159\n"
  "v_mfma_f32_16x16x32_bf16 v[192:195], v[212:215], v[128:131], v[192:195]\n"
  "ds_read_b128 a[192:195], v220 offset:8192\n"
  "v_fma_f32 v160, v160, s38, v208\n"
  "v_mfma_f32_16x16x32_bf16 v[0:3], a[224:227], v[128:131], v[0:3]\n"
  "v_exp_f32 v160, v160\n"
  "v_mfma_f32_16x16x32_bf16 v[4:7], a[228:231], v[128:131], v[4:7]\n"
  "ds_read_b128 a[196:199], v220 offset:8704\n"
  "v_fma_f32 v161, v161, s38, v208\n"
  "v_mfma_f32_16x16x32_bf16 v[8:11], a[232:235], v[128:131], v[8:11]\n"
  "buffer_load_dwordx4 v223, s[12:15], s41 offen offset:1024 lds\n"
  "v_fma_f32 v162, v162, s38, v208\n"
  "v_mfma_f32_16x16x32_bf16 v[12:15], a[236:239], v[128:131], v[12:15]\n"
  "v_exp_f32 v161, v161\n"
  "v_mfma_f32_16x16x32_bf16 v[16:19], a[240:243], v[128:131], v[16:19]\n"
  "v_cvt_pk_bf16_f32 v160, v160, v161\n"
  "v_fma_f32 v163, v163, s38, v208\n"
  "v_mfma_f32_16x16x32_bf16 v[20:23], a[244:247], v[128:131], v[20:23]\n"
  "v_exp_f32 v162, v162\n"
  "v_mfma_f32_16x16x32_bf16 v[24:27], a[248:251], v[128:131], v[24:27]\n"
  "v_exp_f32 v163, v163\n"
  "v_mfma_f32_16x16x32_bf16 v[28:31], a[252:255], v[128:131], v[28:31]\n"
  "ds_read_b128 a[200:203], v220 offset:9216\n"
  "v_cvt_pk_bf16_f32 v161, v162, v163\n"
  "v_mfma_f32_16x16x32_bf16 v[196:199], v[212:215], v[136:139], v[196:199]\n"
  "buffer_load_dwordx4 v224, s[12:15], s41 offen offset:2048 lds\n"
  "v_mfma_f32_16x16x32_bf16 v[32:35], a[224:227], v[136:139], v[32:35]\n"
  "ds_read_b128 a[204:207], v220 offset:9728\n"
  "v_fma_f32 v164, v164, s38, v208\n"
  "v_fma_f32 v165, v165, s38, v208\n"
  "v_mfma_f32_16x16x32_bf16 v[36:39], a[228:231], v[136:139], v[36:39]\n"
  "v_exp_f32 v164, v164\n"
  "v_mfma_f32_16x16x32_bf16 v[40:43], a[232:235], v[136:139], v[40:43]\n"
  "v_exp_f32 v165, v165\n"
  "v_mfma_f32_16x16x32_bf16 v[44:47], a[236:239], v[136:139], v[44:47]\n"
  "ds_read_b128 a[208:211], v220 offset:12288\n"
  "v_cvt_pk_bf16_f32 v162, v164, v165\n"
  "v_mfma_f32_16x16x32_bf16 v[48:51], a[240:243], v[136:139], v[48:51]\n"
  "ds_read_b128 a[212:215], v220 offset:12800\n"
  "v_fma_f32 v166, v166, s38, v208\n"
  "v_mfma_f32_16x16x32_bf16 v[52:55], a[244:247], v[136:139], v[52:55]\n"
  "buffer_load_dwordx4 v225, s[12:15], s41 offen offset:3072 lds\n"
  "v_mfma_f32_16x16x32_bf16 v[56:59], a[248:251], v[136:139], v[56:59]\n"
  "v_exp_f32 v166, v166\n"
  "v_mfma_f32_16x16x32_bf16 v[60:63], a[252:255], v[136:139], v[60:63]\n"
  "ds_read_b128 a[216:219], v220 offset:13312\n"
  "ds_read_b128 a[220:223], v220 offset:13824\n"
  "v_xor_b32 v220, s43, v220\n"
  "v_mfma_f32_16x16x32_bf16 v[200:203], v[212:215], v[144:147], v[200:203]\n"
  "v_fma_f32 v167, v167, s38, v208\n"
  "v_fma_f32 v168, v168, s38, v209\n"
  "v_mfma_f32_16x16x32_bf16 v[64:67], a[224:227], v[144:147], v[64:67]\n"
  "v_exp_f32 v167, v167\n"
  "v_mfma_f32_16x16x32_bf16 v[68:71], a[228:231], v[144:147], v[68:71]\n"
  "v_cvt_pk_bf16_f32 v163, v166, v167\n"
  "v_fma_f32 v169, v169, s38, v209\n"
  "v_mfma_f32_16x16x32_bf16 v[72:75], a[232:235], v[144:147], v[72:75]\n"
  "v_exp_f32 v168, v168\n"
  "v_mfma_f32_16x16x32_bf16 v[76:79], a[236:239], v[144:147], v[76:79]\n"
  "v_exp_f32 v169, v169\n"
  "v_mfma_f32_16x16x32_bf16 v[80:83], a[240:243], v[144:147], v[80:83]\n"
  "v_cvt_pk_bf16_f32 v168, v168, v169\n"
  "v_fma_f32 v170, v170, s38, v209\n"
  "v_mfma_f32_16x16x32_bf16 v[84:87], a[244:247], v[144:147], v[84:87]\n"
  "v_exp_f32 v170, v170\n"
  "v_mfma_f32_16x16x32_bf16 v[88:91], a[248:251], v[144:147], v[88:91]\n"
  "v_fma_f32 v171, v171, s38, v209\n"
  "v_fma_f32 v172, v172, s38, v209\n"
  "v_fma_f32 v173, v173, s38, v209\n"
  "v_mfma_f32_16x16x32_bf16 v[92:95], a[252:255], v[144:147], v[92:95]\n"
  "v_exp_f32 v171, v171\n"
  "v_mfma_f32_16x16x32_bf16 v[204:207], v[212:215], v[152:155], v[204:207]\n"
  "v_cvt_pk_bf16_f32 v169, v170, v171\n"
  "v_fma_f32 v174, v174, s38, v209\n"
  "v_mfma_f32_16x16x32_bf16 v[96:99], a[224:227], v[152:155], v[96:99]\n"
  "v_exp_f32 v172, v172\n"
  "v_mfma_f32_16x16x32_bf16 v[100:103], a[228:231], v[152:155], v[100:103]\n"
  "v_exp_f32 v173, v173\n"
  "v_mfma_f32_16x16x32_bf16 v[104:107], a[232:235], v[152:155], v[104:107]\n"
  "s_mov_b32 s41, s39\n"
  "v_cvt_pk_bf16_f32 v170, v172, v173\n"
  "v_mfma_f32_16x16x32_bf16 v[108:111], a[236:239], v[152:155], v[108:111]\n"
  "s_mov_b32 s14, s10\n"
  "v_fma_f32 v175, v175, s38, v209\n"
  "v_mfma_f32_16x16x32_bf16 v[112:115], a[240:243], v[152:155], v[112:115]\n"
  "s_add_u32 s39, s39, 0x40000\n"
  "v_exp_f32 v174, v174\n"
  "v_mfma_f32_16x16x32_bf16 v[116:119], a[244:247], v[152:155], v[116:119]\n"
  "s_xor_b32 s42, s42, s43\n"
  "v_mfma_f32_16x16x32_bf16 v[120:123], a[248:251], v[152:155], v[120:123]\n"
  "s_xor_b32 s43, s42, s43\n"
  "v_mfma_f32_16x16x32_bf16 v[124:127], a[252:255], v[152:155], v[124:127]\n"
  "s_sub_u32 s44, s44, 1\n"
  "v_exp_f32 v175, v175\n"
  "s_waitcnt vmcnt(8) lgkmcnt(0)\n"
  "v_cvt_pk_bf16_f32 v171, v174, v175\n"
  "s_cbranch_scc1 L_disp\n"
  "L_loop4q:\n"
  "v_mfma_f32_16x16x32_bf16 v[128:131], a[192:195], a[0:3], 0\n"
  "s_barrier\n"
  "ds_read_b64_tr_b16 a[224:225], v221 offset:0\n"
  "v_fma_f32 v176, v176, s38, v210\n"
  "v_mfma_f32_16x16x32_bf16 v[128:131], a[196:199], a[4:7], v[128:131]\n"
  "ds_read_b64_tr_b16 a[226:227], v221 offset:4096\n"
  "v_exp_f32 v176, v176\n"
  "v_mfma_f32_16x16x32_bf16 v[128:131], a[200:203], a[8:11], v[128:131]\n"
  "ds_read_b64_tr_b16 a[228:229], v239 offset:0\n"
  "v_fma_f32 v177, v177, s38, v210\n"
  "v_fma_f32 v178, v178, s38, v210\n"
  "v_mfma_f32_16x16x32_bf16 v[128:131], a[204:207], a[12:15], v[128:131]\n"
  "v_exp_f32 v177, v177\n"
  "v_mfma_f32_16x16x32_bf16 v[132:135], a[208:211], a[0:3], 0\n"
  "ds_read_b64_tr_b16 a[230:231], v239 offset:4096\n"
  "v_cvt_pk_bf16_f32 v176, v176, v177\n"
  "v_fma_f32 v179, v179, s38, v210\n"
  "v_mfma_f32_16x16x32_bf16 v[132:135], a[212:215], a[4:7], v[132:135]\n"
  "buffer_store_dwordx2 v[232:233], v227, s[24:27], s51 offen sc1\n"
  "v_fma_f32 v180, v180, s38, v210\n"
  "v_mfma_f32_16x16x32_bf16 v[132:135], a[216:219], a[8:11], v[132:135]\n"
  "ds_read_b64_tr_b16 a[232:233], v221 offset:512\n"
  "v_exp_f32 v178, v178\n"
  "v_mfma_f32_16x16x32_bf16 v[132:135], a[220:223], a[12:15], v[132:135]\n"
  "ds_read_b64_tr_b16 a[234:235], v221 offset:4608\n"
  "v_exp_f32 v179, v179\n"
  "v_mfma_f32_16x16x32_bf16 v[136:139], a[192:195], a[16:19], 0\n"
  "buffer_load_dwordx2 v[230:231], v227, s[24:27], s50 offen sc1\n"
  "v_cvt_pk_bf16_f32 v177, v178, v179\n"
  "v_mfma_f32_16x16x32_bf16 v[136:139], a[196:199], a[20:23], v[136:139]\n"
  "v_exp_f32 v180, v180\n"
  "v_mfma_f32_16x16x32_bf16 v[136:139], a[200:203], a[24:27], v[136:139]\n"
  "ds_read_b64_tr_b16 a[236:237], v239 offset:512\n"
  "v_fma_f32 v181, v181, s38, v210\n"
  "v_fma_f32 v182, v182, s38, v210\n"
  "v_mfma_f32_16x16x32_bf16 v[136:139], a[204:207], a[28:31], v[136:139]\n"
  "s_add_u32 m0, s82, 0\n"
  "v_exp_f32 v181, v181\n"
  "v_mfma_f32_16x16x32_bf16 v[140:143], a[208:211], a[16:19], 0\n"
  "buffer_load_dwordx4 v222, s[16:19], s28 offen offset:0 lds\n"
  "v_cvt_pk_bf16_f32 v178, v180, v181\n"
  "v_mfma_f32_16x16x32_bf16 v[140:143], a[212:215], a[20:23], v[140:143]\n"
  "ds_read_b64_tr_b16 a[238:239], v239 offset:4608\n"
  "v_exp_f32 v182, v182\n"
  "v_mfma_f32_16x16x32_bf16 v[140:143], a[216:219], a[24:27], v[140:143]\n"
  "ds_read_b64_tr_b16 a[240:241], v221 offset:1024\n"
  "v_fma_f32 v183, v183, s38, v210\n"
  "v_fma_f32 v184, v184, s38, v211\n"
  "v_mfma_f32_16x16x32_bf16 v[140:143], a[220:223], a[28:31], v[140:143]\n"
  "buffer_load_dwordx4 v223, s[16:19], s28 offen offset:1024 lds\n"
  "v_mfma_f32_16x16x32_bf16 v[144:147], a[192:195], a[32:35], 0\n"
  "ds_read_b64_tr_b16 a[242:243], v221 offset:5120\n"
  "v_exp_f32 v183, v183\n"
  "v_mfma_f32_16x16x32_bf16 v[144:147], a[196:199], a[36:39], v[144:147]\n"
  "ds_read_b64_tr_b16 a[244:245], v239 offset:1024\n"
  "v_cvt_pk_bf16_f32 v179, v182, v183\n"
  "v_fma_f32 v185, v185, s38, v211\n"
  "v_mfma_f32_16x16x32_bf16 v[144:147], a[200:203], a[40:43], v[144:147]\n"
  "ds_read_b64_tr_b16 a[246:247], v239 offset:5120\n"
  "v_exp_f32 v184, v184\n"
  "v_mfma_f32_16x16x32_bf16 v[144:147], a[204:207], a[44:47], v[144:147]\n"
  "buffer_load_dwordx4 v224, s[16:19], s28 offen offset:2048 lds\n"
  "v_fma_f32 v186, v186, s38, v211\n"
  "v_mfma_f32_16x16x32_bf16 v[148:151], a[208:211], a[32:35], 0\n"
  "ds_read_b64_tr_b16 a[248:249], v221 offset:1536\n"
  "v_exp_f32 v185, v185\n"
  "v_mfma_f32_16x16x32_bf16 v[148:151], a[212:215], a[36:39], v[148:151]\n"
  "ds_read_b64_tr_b16 a[250:251], v221 offset:5632\n"
  "v_cvt_pk_bf16_f32 v184, v184, v185\n"
  "v_fma_f32 v187, v187, s38, v211\n"
  "v_mfma_f32_16x16x32_bf16 v[148:151], a[216:219], a[40:43], v[148:151]\n"
  "buffer_load_dwordx4 v225, s[16:19], s28 offen offset:3072 lds\n"
  "v_mfma_f32_16x16x32_bf16 v[148:151], a[220:223], a[44:47], v[148:151]\n"
  "ds_read_b64_tr_b16 a[252:253], v239 offset:1536\n"
  "v_exp_f32 v186, v186\n"
  "v_mfma_f32_16x16x32_bf16 v[152:155], a[192:195], a[48:51], 0\n"
  "ds_read_b64_tr_b16 a[254:255], v239 offset:5632\n"
  "v_exp_f32 v187, v187\n"
  "v_mfma_f32_16x16x32_bf16 v[152:155], a[196:199], a[52:55], v[152:155]\n"
  "s_add_u32 m0, s82, 4096\n"
  "v_cvt_pk_bf16_f32 v185, v186, v187\n"
  "v_fma_f32 v188, v188, s38, v211\n"
  "v_mfma_f32_16x16x32_bf16 v[152:155], a[200:203], a[56:59], v[152:155]\n"
  "buffer_load_dwordx4 v222, s[16:19], s29 offen offset:0 lds\n"
  "v_fma_f32 v189, v189, s38, v211\n"
  "v_mfma_f32_16x16x32_bf16 v[152:155], a[204:207], a[60:63], v[152:155]\n"
  "v_exp_f32 v188, v188\n"
  "v_fma_f32 v190, v190, s38, v211\n"
  "v_mfma_f32_16x16x32_bf16 v[156:159], a[208:211], a[48:51], 0\n"
  "v_exp_f32 v189, v189\n"
  "v_mfma_f32_16x16x32_bf16 v[156:159], a[212:215], a[52:55], v[156:159]\n"
  "buffer_load_dwordx4 v223, s[16:19], s29 offen offset:1024 lds\n"
  "v_cvt_pk_bf16_f32 v186, v188, v189\n"
  "v_mfma_f32_16x16x32_bf16 v[156:159], a[216:219], a[56:59], v[156:159]\n"
  "v_exp_f32 v190, v190\n"
  "v_fma_f32 v191, v191, s38, v211\n"
  "v_mfma_f32_16x16x32_bf16 v[156:159], a[220:223], a[60:63], v[156:159]\n"
  "v_exp_f32 v191, v191\n"
  "s_waitcnt lgkmcnt(0)\n"
  "v_cvt_pk_bf16_f32 v187, v190, v191\n"
  "v_mfma_f32_16x16x32_bf16 v[192:195], v[216:219], v[160:163], v[192:195]\n"
  "ds_read_b128 a[192:195], v220 offset:0\n"
  "v_fma_f32 v128, v128, s38, v208\n"
  "v_mfma_f32_16x16x32_bf16 v[0:3], a[224:227], v[160:163], v[0:3]\n"
  "buffer_load_dwordx4 v224, s[16:19], s29 offen offset:2048 lds\n"
  "v_fma_f32 v129, v129, s38, v208\n"
  "v_mfma_f32_16x16x32_bf16 v[4:7], a[228:231], v[160:163], v[4:7]\n"
  "v_exp_f32 v128, v128\n"
  "v_mfma_f32_16x16x32_bf16 v[8:11], a[232:235], v[160:163], v[8:11]\n"
  "ds_read_b128 a[196:199], v220 offset:512\n"
  "v_exp_f32 v129, v129\n"
  "v_mfma_f32_16x16x32_bf16 v[12:15], a[236:239], v[160:163], v[12:15]\n"
  "buffer_load_dwordx4 v225, s[16:19], s29 offen offset:3072 lds\n"
  "v_mfma_f32_16x16x32_bf16 v[16:19], a[240:243], v[160:163], v[16:19]\n"
  "ds_read_b128 a[200:203], v220 offset:1024\n"
  "v_cvt_pk_bf16_f32 v128, v128, v129\n"
  "v_mfma_f32_16x16x32_bf16 v[20:23], a[244:247], v[160:163], v[20:23]\n"
  "ds_read_b128 a[204:207], v220 offset:1536\n"
  "v_fma_f32 v130, v130, s38, v208\n"
  "v_fma_f32 v131, v131, s38, v208\n"
  "v_mfma_f32_16x16x32_bf16 v[24:27], a[248:251], v[160:163], v[24:27]\n"
  "s_add_u32 m0, s82, 8192\n"
  "v_fma_f32 v132, v132, s38, v208\n"
  "v_mfma_f32_16x16x32_bf16 v[28:31], a[252:255], v[160:163], v[28:31]\n"
  "buffer_load_dwordx4 v222, s[16:19], s30 offen offset:0 lds\n"
  "v_mfma_f32_16x16x32_bf16 v[196:199], v[216:219], v[168:171], v[196:199]\n"
  "ds_read_b128 a[208:211], v220 offset:4096\n"
  "v_exp_f32 v130, v130\n"
  "v_mfma_f32_16x16x32_bf16 v[32:35], a[224:227], v[168:171], v[32:35]\n"
  "v_exp_f32 v131, v131\n"
  "v_mfma_f32_16x16x32_bf16 v[36:39], a[228:231], v[168:171], v[36:39]\n"
  "buffer_load_dwordx4 v223, s[16:19], s30 offen offset:1024 lds\n"
  "v_cvt_pk_bf16_f32 v129, v130, v131\n"
  "v_mfma_f32_16x16x32_bf16 v[40:43], a[232:235], v[168:171], v[40:43]\n"
  "v_exp_f32 v132, v132\n"
  "v_mfma_f32_16x16x32_bf16 v[44:47], a[236:239], v[168:171], v[44:47]\n"
  "ds_read_b128 a[212:215], v220 offset:4608\n"
  "v_fma_f32 v133, v133, s38, v208\n"
  "v_mfma_f32_16x16x32_bf16 v[48:51], a[240:243], v[168:171], v[48:51]\n"
  "ds_read_b128 a[216:219], v220 offset:5120\n"
  "v_exp_f32 v133, v133\n"
  "v_mfma_f32_16x16x32_bf16 v[52:55], a[244:247], v[168:171], v[52:55]\n"
  "buffer_load_dwordx4 v224, s[16:19], s30 offen offset:2048 lds\n"
  "v_mfma_f32_16x16x32_bf16 v[56:59], a[248:251], v[168:171], v[56:59]\n"
  "ds_read_b128 a[220:223], v220 offset:5632\n"
  "v_cvt_pk_bf16_f32 v130, v132, v133\n"
  "v_fma_f32 v134, v134, s38, v208\n"
  "v_mfma_f32_16x16x32_bf16 v[60:63], a[252:255], v[168:171], v[60:63]\n"
  "v_exp_f32 v134, v134\n"
  "v_mfma_f32_16x16x32_bf16 v[200:203], v[216:219], v[176:179], v[200:203]\n"
  "buffer_load_dwordx4 v225, s[16:19], s30 offen offset:3072 lds\n"
  "v_mfma_f32_16x16x32_bf16 v[64:67], a[224:227], v[176:179], v[64:67]\n"
  "v_fma_f32 v135, v135, s38, v208\n"
  "v_fma_f32 v136, v136, s38, v209\n"
  "v_fma_f32 v137, v137, s38, v209\n"
  "v_mfma_f32_16x16x32_bf16 v[68:71], a[228:231], v[176:179], v[68:71]\n"
  "v_exp_f32 v135, v135\n"
  "v_mfma_f32_16x16x32_bf16 v[72:75], a[232:235], v[176:179], v[72:75]\n"
  "s_add_u32 m0, s82, 12288\n"
  "v_cvt_pk_bf16_f32 v131, v134, v135\n"
  "v_fma_f32 v138, v138, s38, v209\n"
  "v_mfma_f32_16x16x32_bf16 v[76:79], a[236:239], v[176:179], v[76:79]\n"
  "buffer_load_dwordx4 v222, s[16:19], s31 offen offset:0 lds\n"
  "v_mfma_f32_16x16x32_bf16 v[80:83], a[240:243], v[176:179], v[80:83]\n"
  "v_exp_f32 v136, v136\n"
  "v_mfma_f32_16x16x32_bf16 v[84:87], a[244:247], v[176:179], v[84:87]\n"
  "v_exp_f32 v137, v137\n"
  "v_fma_f32 v139, v139, s38, v209\n"
  "v_mfma_f32_16x16x32_bf16 v[88:91], a[248:251], v[176:179], v[88:91]\n"
  "buffer_load_dwordx4 v223, s[16:19], s31 offen offset:1024 lds\n"
  "v_mfma_f32_16x16x32_bf16 v[92:95], a[252:255], v[176:179], v[92:95]\n"
  "v_cvt_pk_bf16_f32 v136, v136, v137\n"
  "v_fma_f32 v140, v140, s38, v209\n"
  "v_mfma_f32_16x16x32_bf16 v[204:207], v[216:219], v[184:187], v[204:207]\n"
  "v_exp_f32 v138, v138\n"
  "v_fma_f32 v141, v141, s38, v209\n"
  "v_mfma_f32_16x16x32_bf16 v[96:99], a[224:227], v[184:187], v[96:99]\n"
  "v_exp_f32 v139, v139\n"
  "v_mfma_f32_16x16x32_bf16 v[100:103], a[228:231], v[184:187], v[100:103]\n"
  "buffer_load_dwordx4 v224, s[16:19], s31 offen offset:2048 lds\n"
  "v_cvt_pk_bf16_f32 v137, v138, v139\n"
  "v_mfma_f32_16x16x32_bf16 v[104:107], a[232:235], v[184:187], v[104:107]\n"
  "v_exp_f32 v140, v140\n"
  "v_mfma_f32_16x16x32_bf16 v[108:111], a[236:239], v[184:187], v[108:111]\n"
  "v_exp_f32 v141, v141\n"
  "v_mfma_f32_16x16x32_bf16 v[112:115], a[240:243], v[184:187], v[112:115]\n"
  "buffer_load_dwordx4 v225, s[16:19], s31 offen offset:3072 lds\n"
  "v_cvt_pk_bf16_f32 v138, v140, v141\n"
  "v_mfma_f32_16x16x32_bf16 v[116:119], a[244:247], v[184:187], v[116:119]\n"
  "v_fma_f32 v142, v142, s38, v209\n"
  "v_fma_f32 v143, v143, s38, v209\n"
  "v_mfma_f32_16x16x32_bf16 v[120:123], a[248:251], v[184:187], v[120:123]\n"
  "v_exp_f32 v142, v142\n"
  "v_mfma_f32_16x16x32_bf16 v[124:127], a[252:255], v[184:187], v[124:127]\n"
  "v_exp_f32 v143, v143\n"
  "s_waitcnt lgkmcnt(0)\n"
  "v_cvt_pk_bf16_f32 v139, v142, v143\n"
  "s_branch L_loop4_G2\n"
  "L_loop4:\n"
  "v_mfma_f32_16x16x32_bf16 v[128:131], a[192:195], a[0:3], 0\n"
  "s_barrier\n"
  "ds_read_b64_tr_b16 a[224:225], v221 offset:0\n"
  "v_mfma_f32_16x16x32_bf16 v[128:131], a[196:199], a[4:7], v[128:131]\n"
  "ds_read_b64_tr_b16 a[226:227], v221 offset:4096\n"
  "v_fma_f32 v176, v176, s38, v210\n"
  "v_fma_f32 v177, v177, s38, v210\n"
  "v_mfma_f32_16x16x32_bf16 v[128:131], a[200:203], a[8:11], v[128:131]\n"
  "v_exp_f32 v176, v176\n"
  "v_mfma_f32_16x16x32_bf16 v[128:131], a[204:207], a[12:15], v[128:131]\n"
  "v_exp_f32 v177, v177\n"
  "v_mfma_f32_16x16x32_bf16 v[132:135], a[208:211], a[0:3], 0\n"
  "ds_read_b64_tr_b16 a[228:229], v239 offset:0\n"
  "v_cvt_pk_bf16_f32 v176, v176, v177\n"
  "v_mfma_f32_16x16x32_bf16 v[132:135], a[212:215], a[4:7], v[132:135]\n"
  "ds_read_b64_tr_b16 a[230:231], v239 offset:4096\n"
  "v_fma_f32 v178, v178, s38, v210\n"
  "v_fma_f32 v179, v179, s38, v210\n"
  "v_mfma_f32_16x16x32_bf16 v[132:135], a[216:219], a[8:11], v[132:135]\n"
  "v_exp_f32 v178, v178\n"
  "v_mfma_f32_16x16x32_bf16 v[132:135], a[220:223], a[12:15], v[132:135]\n"
  "v_exp_f32 v179, v179\n"
  "v_mfma_f32_16x16x32_bf16 v[136:139], a[192:195], a[16:19], 0\n"
  "ds_read_b64_tr_b16 a[232:233], v221 offset:512\n"
  "v_cvt_pk_bf16_f32 v177, v178, v179\n"
  "v_fma_f32 v180, v180, s38, v210\n"
  "v_mfma_f32_16x16x32_bf16 v[136:139], a[196:199], a[20:23], v[136:139]\n"
  "v_exp_f32 v180, v180\n"
  "v_mfma_f32_16x16x32_bf16 v[136:139], a[200:203], a[24:27], v[136:139]\n"
  "ds_read_b64_tr_b16 a[234:235], v221 offset:4608\n"
  "v_fma_f32 v181, v181, s38, v210\n"
  "v_mfma_f32_16x16x32_bf16 v[136:139], a[204:207], a[28:31], v[136:139]\n"
  "v_exp_f32 v181, v181\n"
  "v_mfma_f32_16x16x32_bf16 v[140:143], a[208:211], a[16:19], 0\n"
  "ds_read_b64_tr_b16 a[236:237], v239 offset:512\n"
  "v_cvt_pk_bf16_f32 v178, v180, v181\n"
  "v_fma_f32 v182, v182, s38, v210\n"
  "v_mfma_f32_16x16x32_bf16 v[140:143], a[212:215], a[20:23], v[140:143]\n"
  "v_exp_f32 v182, v182\n"
  "v_mfma_f32_16x16x32_bf16 v[140:143], a[216:219], a[24:27], v[140:143]\n"
  "ds_read_b64_tr_b16 a[238:239], v239 offset:4608\n"
  "v_fma_f32 v183, v183, s38, v210\n"
  "v_mfma_f32_16x16x32_bf16 v[140:143], a[220:223], a[28:31], v[140:143]\n"
  "v_exp_f32 v183, v183\n"
  "v_mfma_f32_16x16x32_bf16 v[144:147], a[192:195], a[32:35], 0\n"
  "ds_read_b64_tr_b16 a[240:241], v221 offset:1024\n"
  "v_cvt_pk_bf16_f32 v179, v182, v183\n"
  "v_fma_f32 v184, v184, s38, v211\n"
  "v_mfma_f32_16x16x32_bf16 v[144:147], a[196:199], a[36:39], v[144:147]\n"
  "v_exp_f32 v184, v184\n"
  "v_mfma_f32_16x16x32_bf16 v[144:147], a[200:203], a[40:43], v[144:147]\n"
  "ds_read_b64_tr_b16 a[242:243], v221 offset:5120\n"
  "v_fma_f32 v185, v185, s38, v211\n"
  "v_mfma_f32_16x16x32_bf16 v[144:147], a[204:207], a[44:47], v[144:147]\n"
  "ds_read_b64_tr_b16 a[244:245], v239 offset:1024\n"
  "v_exp_f32 v185, v185\n"
  "v_mfma_f32_16x16x32_bf16 v[148:151], a[208:211], a[32:35], 0\n"
  "ds_read_b64_tr_b16 a[246:247], v239 offset:5120\n"
  "v_cvt_pk_bf16_f32 v184, v184, v185\n"
  "v_mfma_f32_16x16x32_bf16 v[148:151], a[212:215], a[36:39], v[148:151]\n"
  "ds_read_b64_tr_b16 a[248:249], v221 offset:1536\n"
  "v_fma_f32 v186, v186, s38, v211\n"
  "v_mfma_f32_16x16x32_bf16 v[148:151], a[216:219], a[40:43], v[148:151]\n"
  "v_exp_f32 v186, v186\n"
  "v_mfma_f32_16x16x32_bf16 v[148:151], a[220:223], a[44:47], v[148:151]\n"
  "ds_read_b64_tr_b16 a[250:251], v221 offset:5632\n"
  "ds_read_b64_tr_b16 a[252:253], v239 offset:1536\n"
  "v_fma_f32 v187, v187, s38, v211\n"
  "v_mfma_f32_16x16x32_bf16 v[152:155], a[192:195], a[48:51], 0\n"
  "ds_read_b64_tr_b16 a[254:255], v239 offset:5632\n"
  "v_fma_f32 v188, v188, s38, v211\n"
  "v_mfma_f32_16x16x32_bf16 v[152:155], a[196:199], a[52:55], v[152:155]\n"
  "v_exp_f32 v187, v187\n"
  "v_mfma_f32_16x16x32_bf16 v[152:155], a[200:203], a[56:59], v[152:155]\n"
  "v_cvt_pk_bf16_f32 v185, v186, v187\n"
  "v_exp_f32 v188, v188\n"
  "v_mfma_f32_16x16x32_bf16 v[152:155], a[204:207], a[60:63], v[152:155]\n"
  "v_fma_f32 v189, v189, s38, v211\n"
  "v_fma_f32 v190, v190, s38, v211\n"
  "v_mfma_f32_16x16x32_bf16 v[156:159], a[208:211], a[48:51], 0\n"
  "v_exp_f32 v189, v189\n"
  "v_mfma_f32_16x16x32_bf16 v[156:159], a[212:215], a[52:55], v[156:159]\n"
  "v_cvt_pk_bf16_f32 v186, v188, v189\n"
  "v_fma_f32 v191, v191, s38, v211\n"
  "v_mfma_f32_16x16x32_bf16 v[156:159], a[216:219], a[56:59], v[156:159]\n"
  "v_exp_f32 v190, v190\n"
  "v_mfma_f32_16x16x32_bf16 v[156:159], a[220:223], a[60:63], v[156:159]\n"
  "v_exp_f32 v191, v191\n"
  "s_waitcnt lgkmcnt(0)\n"
  "v_cvt_pk_bf16_f32 v187, v190, v191\n"
  "v_mfma_f32_16x16x32_bf16 v[192:195], v[216:219], v[160:163], v[192:195]\n"
  "ds_read_b128 a[192:195], v220 offset:0\n"
  "v_fma_f32 v128, v128, s38, v208\n"
  "v_mfma_f32_16x16x32_bf16 v[0:3], a[224:227], v[160:163], v[0:3]\n"
  "v_exp_f32 v128, v128\n"
  "v_mfma_f32_16x16x32_bf16 v[4:7], a[228:231], v[160:163], v[4:7]\n"
  "ds_read_b128 a[196:199], v220 offset:512\n"
  "v_fma_f32 v129, v129, s38, v208\n"
  "v_mfma_f32_16x16x32_bf16 v[8:11], a[232:235], v[160:163], v[8:11]\n"
  "v_exp_f32 v129, v129\n"
  "v_mfma_f32_16x16x32_bf16 v[12:15], a[236:239], v[160:163], v[12:15]\n"
  "ds_read_b128 a[200:203], v220 offset:1024\n"
  "v_cvt_pk_bf16_f32 v128, v128, v129\n"
  "v_mfma_f32_16x16x32_bf16 v[16:19], a[240:243], v[160:163], v[16:19]\n"
  "ds_read_b128 a[204:207], v220 offset:1536\n"
  "v_fma_f32 v130, v130, s38, v208\n"
  "v_mfma_f32_16x16x32_bf16 v[20:23], a[244:247], v[160:163], v[20:23]\n"
  "v_exp_f32 v130, v130\n"
  "v_mfma_f32_16x16x32_bf16 v[24:27], a[248:251], v[160:163], v[24:27]\n"
  "ds_read_b128 a[208:211], v220 offset:4096\n"
  "v_fma_f32 v131, v131, s38, v208\n"
  "v_mfma_f32_16x16x32_bf16 v[28:31], a[252:255], v[160:163], v[28:31]\n"
  "v_exp_f32 v131, v131\n"
  "v_mfma_f32_16x16x32_bf16 v[196:199], v[216:219], v[168:171], v[196:199]\n"
  "ds_read_b128 a[212:215], v220 offset:4608\n"
  "v_cvt_pk_bf16_f32 v129, v130, v131\n"
  "v_mfma_f32_16x16x32_bf16 v[32:35], a[224:227], v[168:171], v[32:35]\n"
  "ds_read_b128 a[216:219], v220 offset:5120\n"
  "v_fma_f32 v132, v132, s38, v208\n"
  "v_mfma_f32_16x16x32_bf16 v[36:39], a[228:231], v[168:171], v[36:39]\n"
  "v_exp_f32 v132, v132\n"
  "v_mfma_f32_16x16x32_bf16 v[40:43], a[232:235], v[168:171], v[40:43]\n"
  "ds_read_b128 a[220:223], v220 offset:5632\n"
  "v_fma_f32 v133, v133, s38, v208\n"
  "v_mfma_f32_16x16x32_bf16 v[44:47], a[236:239], v[168:171], v[44:47]\n"
  "v_exp_f32 v133, v133\n"
  "v_mfma_f32_16x16x32_bf16 v[48:51], a[240:243], v[168:171], v[48:51]\n"
  "v_cvt_pk_bf16_f32 v130, v132, v133\n"
  "v_fma_f32 v134, v134, s38, v208\n"
  "v_mfma_f32_16x16x32_bf16 v[52:55], a[244:247], v[168:171], v[52:55]\n"
  "v_exp_f32 v134, v134\n"
  "v_mfma_f32_16x16x32_bf16 v[56:59], a[248:251], v[168:171], v[56:59]\n"
  "buffer_store_dwordx2 v[232:233], v227, s[24:27], s51 offen sc1\n"
  "v_mfma_f32_16x16x32_bf16 v[60:63], a[252:255], v[168:171], v[60:63]\n"
  "v_fma_f32 v135, v135, s38, v208\n"
  "v_fma_f32 v136, v136, s38, v209\n"
  "v_mfma_f32_16x16x32_bf16 v[200:203], v[216:219], v[176:179], v[200:203]\n"
  "v_exp_f32 v135, v135\n"
  "v_mfma_f32_16x16x32_bf16 v[64:67], a[224:227], v[176:179], v[64:67]\n"
  "v_cvt_pk_bf16_f32 v131, v134, v135\n"
  "v_fma_f32 v137, v137, s38, v209\n"
  "v_mfma_f32_16x16x32_bf16 v[68:71], a[228:231], v[176:179], v[68:71]\n"
  "v_exp_f32 v136, v136\n"
  "v_mfma_f32_16x16x32_bf16 v[72:75], a[232:235], v[176:179], v[72:75]\n"
  "v_exp_f32 v137, v137\n"
  "v_mfma_f32_16x16x32_bf16 v[76:79], a[236:239], v[176:179], v[76:79]\n"
  "v_cvt_pk_bf16_f32 v136, v136, v137\n"
  "v_fma_f32 v138, v138, s38, v209\n"
  "v_mfma_f32_16x16x32_bf16 v[80:83], a[240:243], v[176:179], v[80:83]\n"
  "buffer_load_dwordx2 v[230:231], v227, s[24:27], s50 offen sc1\n"
  "v_mfma_f32_16x16x32_bf16 v[84:87], a[244:247], v[176:179], v[84:87]\n"
  "v_exp_f32 v138, v138\n"
  "v_mfma_f32_16x16x32_bf16 v[88:91], a[248:251], v[176:179], v[88:91]\n"
  "v_fma_f32 v139, v139, s38, v209\n"
  "v_fma_f32 v140, v140, s38, v209\n"
  "v_mfma_f32_16x16x32_bf16 v[92:95], a[252:255], v[176:179], v[92:95]\n"
  "v_exp_f32 v139, v139\n"
  "v_mfma_f32_16x16x32_bf16 v[204:207], v[216:219], v[184:187], v[204:207]\n"
  "v_cvt_pk_bf16_f32 v137, v138, v139\n"
  "v_fma_f32 v141, v141, s38, v209\n"
  "v_mfma_f32_16x16x32_bf16 v[96:99], a[224:227], v[184:187], v[96:99]\n"
  "v_exp_f32 v140, v140\n"
  "v_mfma_f32_16x16x32_bf16 v[100:103], a[228:231], v[184:187], v[100:103]\n"
  "v_exp_f32 v141, v141\n"
  "v_mfma_f32_16x16x32_bf16 v[104:107], a[232:235], v[184:187], v[104:107]\n"
  "v_cvt_pk_bf16_f32 v138, v140, v141\n"
  "v_fma_f32 v142, v142, s38, v209\n"
  "v_mfma_f32_16x16x32_bf16 v[108:111], a[236:239], v[184:187], v[108:111]\n"
  "v_exp_f32 v142, v142\n"
  "v_mfma_f32_16x16x32_bf16 v[112:115], a[240:243], v[184:187], v[112:115]\n"
  "v_fma_f32 v143, v143, s38, v209\n"
  "v_mfma_f32_16x16x32_bf16 v[116:119], a[244:247], v[184:187], v[116:119]\n"
  "v_exp_f32 v143, v143\n"
  "v_mfma_f32_16x16x32_bf16 v[120:123], a[248:251], v[184:187], v[120:123]\n"
  "v_cvt_pk_bf16_f32 v139, v142, v143\n"
  "v_mfma_f32_16x16x32_bf16 v[124:127], a[252:255], v[184:187], v[124:127]\n"
  "s_waitcnt lgkmcnt(0)\n"
  "L_loop4_G2:\n"
  "v_mfma_f32_16x16x32_bf16 v[160:163], a[192:195], a[0:3], 0\n"
  "ds_read_b64_tr_b16 a[224:225], v221 offset:8192\n"
  "v_fma_f32 v144, v144, s38, v210\n"
  "v_fma_f32 v145, v145, s38, v210\n"
  "v_mfma_f32_16x16x32_bf16 v[160:163], a[196:199], a[4:7], v[160:163]\n"
  "v_exp_f32 v144, v144\n"
  "v_mfma_f32_16x16x32_bf16 v[160:163], a[200:203], a[8:11], v[160:163]\n"
  "ds_read_b64_tr_b16 a[226:227], v221 offset:12288\n"
  "v_exp_f32 v145, v145\n"
  "v_mfma_f32_16x16x32_bf16 v[160:163], a[204:207], a[12:15], v[160:163]\n"
  "ds_read_b64_tr_b16 a[228:229], v239 offset:8192\n"
  "v_cvt_pk_bf16_f32 v144, v144, v145\n"
  "v_fma_f32 v146, v146, s38, v210\n"
  "v_mfma_f32_16x16x32_bf16 v[164:167], a[208:211], a[0:3], 0\n"
  "v_exp_f32 v146, v146\n"
  "v_mfma_f32_16x16x32_bf16 v[164:167], a[212:215], a[4:7], v[164:167]\n"
  "s_or_b32 m0, s45, s43\n"
  "v_fma_f32 v147, v147, s38, v210\n"
  "v_fma_f32 v148, v148, s38, v210\n"
  "v_mfma_f32_16x16x32_bf16 v[164:167], a[216:219], a[8:11], v[164:167]\n"
  "buffer_load_dwordx4 v222, s[8:11], s39 offen offset:0 lds\n"
  "v_fma_f32 v149, v149, s38, v210\n"
  "v_mfma_f32_16x16x32_bf16 v[164:167], a[220:223], a[12:15], v[164:167]\n"
  "v_exp_f32 v147, v147\n"
  "v_mfma_f32_16x16x32_bf16 v[168:171], a[192:195], a[16:19], 0\n"
  "ds_read_b64_tr_b16 a[230:231], v239 offset:12288\n"
  "v_cvt_pk_bf16_f32 v145, v146, v147\n"
  "v_fma_f32 v150, v150, s38, v210\n"
  "v_mfma_f32_16x16x32_bf16 v[168:171], a[196:199], a[20:23], v[168:171]\n"
  "ds_read_b64_tr_b16 a[232:233], v221 offset:8704\n"
  "v_exp_f32 v148, v148\n"
  "v_mfma_f32_16x16x32_bf16 v[168:171], a[200:203], a[24:27], v[168:171]\n"
  "ds_read_b64_tr_b16 a[234:235], v221 offset:12800\n"
  "v_exp_f32 v149, v149\n"
  "v_mfma_f32_16x16x32_bf16 v[168:171], a[204:207], a[28:31], v[168:171]\n"
  "v_cvt_pk_bf16_f32 v146, v148, v149\n"
  "v_fma_f32 v151, v151, s38, v210\n"
  "v_mfma_f32_16x16x32_bf16 v[172:175], a[208:211], a[16:19], 0\n"
  "buffer_load_dwordx4 v223, s[8:11], s39 offen offset:1024 lds\n"
  "v_fma_f32 v152, v152, s38, v211\n"
  "v_mfma_f32_16x16x32_bf16 v[172:175], a[212:215], a[20:23], v[172:175]\n"
  "ds_read_b64_tr_b16 a[236:237], v239 offset:8704\n"
  "v_exp_f32 v150, v150\n"
  "v_mfma_f32_16x16x32_bf16 v[172:175], a[216:219], a[24:27], v[172:175]\n"
  "v_exp_f32 v151, v151\n"
  "v_mfma_f32_16x16x32_bf16 v[172:175], a[220:223], a[28:31], v[172:175]\n"
  "ds_read_b64_tr_b16 a[238:239], v239 offset:12800\n"
  "v_cvt_pk_bf16_f32 v147, v150, v151\n"
  "v_fma_f32 v153, v153, s38, v211\n"
  "v_mfma_f32_16x16x32_bf16 v[176:179], a[192:195], a[32:35], 0\n"
  "ds_read_b64_tr_b16 a[240:241], v221 offset:9216\n"
  "v_exp_f32 v152, v152\n"
  "v_mfma_f32_16x16x32_bf16 v[176:179], a[196:199], a[36:39], v[176:179]\n"
  "buffer_load_dwordx4 v224, s[8:11], s39 offen offset:2048 lds\n"
  "v_mfma_f32_16x16x32_bf16 v[176:179], a[200:203], a[40:43], v[176:179]\n"
  "ds_read_b64_tr_b16 a[242:243], v221 offset:13312\n"
  "v_exp_f32 v153, v153\n"
  "v_mfma_f32_16x16x32_bf16 v[176:179], a[204:207], a[44:47], v[176:179]\n"
  "ds_read_b64_tr_b16 a[244:245], v239 offset:9216\n"
  "v_cvt_pk_bf16_f32 v152, v152, v153\n"
  "v_fma_f32 v154, v154, s38, v211\n"
  "v_mfma_f32_16x16x32_bf16 v[180:183], a[208:211], a[32:35], 0\n"
  "v_exp_f32 v154, v154\n"
  "v_mfma_f32_16x16x32_bf16 v[180:183], a[212:215], a[36:39], v[180:183]\n"
  "ds_read_b64_tr_b16 a[246:247], v239 offset:13312\n"
  "ds_read_b64_tr_b16 a[248:249], v221 offset:9728\n"
  "v_fma_f32 v155, v155, s38, v211\n"
  "v_mfma_f32_16x16x32_bf16 v[180:183], a[216:219], a[40:43], v[180:183]\n"
  "ds_read_b64_tr_b16 a[250:251], v221 offset:13824\n"
  "v_exp_f32 v155, v155\n"
  "v_mfma_f32_16x16x32_bf16 v[180:183], a[220:223], a[44:47], v[180:183]\n"
  "buffer_load_dwordx4 v225, s[8:11], s39 offen offset:3072 lds\n"
  "v_mfma_f32_16x16x32_bf16 v[184:187], a[192:195], a[48:51], 0\n"
  "ds_read_b64_tr_b16 a[252:253], v239 offset:9728\n"
  "ds_read_b64_tr_b16 a[254:255], v239 offset:13824\n"
  "v_cvt_pk_bf16_f32 v153, v154, v155\n"
  "v_mfma_f32_16x16x32_bf16 v[184:187], a[196:199], a[52:55], v[184:187]\n"
  "v_xor_b32 v221, s43, v221\n"
  "v_xor_b32 v239, s43, v239\n"
  "v_fma_f32 v156, v156, s38, v211\n"
  "v_mfma_f32_16x16x32_bf16 v[184:187], a[200:203], a[56:59], v[184:187]\n"
  "v_exp_f32 v156, v156\n"
  "v_fma_f32 v157, v157, s38, v211\n"
  "v_mfma_f32_16x16x32_bf16 v[184:187], a[204:207], a[60:63], v[184:187]\n"
  "v_exp_f32 v157, v157\n"
  "v_mfma_f32_16x16x32_bf16 v[188:191], a[208:211], a[48:51], 0\n"
  "s_or_b32 m0, s46, s43\n"
  "v_cvt_pk_bf16_f32 v154, v156, v157\n"
  "v_fma_f32 v158, v158, s38, v211\n"
  "v_mfma_f32_16x16x32_bf16 v[188:191], a[212:215], a[52:55], v[188:191]\n"
  "buffer_load_dwordx4 v222, s[12:15], s41 offen offset:0 lds\n"
  "v_fma_f32 v159, v159, s38, v211\n"
  "v_mfma_f32_16x16x32_bf16 v[188:191], a[216:219], a[56:59], v[188:191]\n"
  "v_exp_f32 v158, v158\n"
  "v_mfma_f32_16x16x32_bf16 v[188:191], a[220:223], a[60:63], v[188:191]\n"
  "v_exp_f32 v159, v159\n"
  "s_waitcnt lgkmcnt(0)\n"
  "v_cvt_pk_bf16_f32 v155, v158, v159\n"
  "v_mfma_f32_16x16x32_bf16 v[192:195], v[212:215], v[128:131], v[192:195]\n"
  "ds_read_b128 a[192:195], v220 offset:8192\n"
  "v_fma_f32 v160, v160, s38, v208\n"
  "v_mfma_f32_16x16x32_bf16 v[0:3], a[224:227], v[128:131], v[0:3]\n"
  "v_exp_f32 v160, v160\n"
  "v_mfma_f32_16x16x32_bf16 v[4:7], a[228:231], v[128:131], v[4:7]\n"
  "ds_read_b128 a[196:199], v220 offset:8704\n"
  "v_fma_f32 v161, v161, s38, v208\n"
  "v_mfma_f32_16x16x32_bf16 v[8:11], a[232:235], v[128:131], v[8:11]\n"
  "buffer_load_dwordx4 v223, s[12:15], s41 offen offset:1024 lds\n"
  "v_fma_f32 v162, v162, s38, v208\n"
  "v_mfma_f32_16x16x32_bf16 v[12:15], a[236:239], v[128:131], v[12:15]\n"
  "v_exp_f32 v161, v161\n"
  "v_mfma_f32_16x16x32_bf16 v[16:19], a[240:243], v[128:131], v[16:19]\n"
  "v_cvt_pk_bf16_f32 v160, v160, v161\n"
  "v_fma_f32 v163, v163, s38, v208\n"
  "v_mfma_f32_16x16x32_bf16 v[20:23], a[244:247], v[128:131], v[20:23]\n"
  "v_exp_f32 v162, v162\n"
  "v_mfma_f32_16x16x32_bf16 v[24:27], a[248:251], v[128:131], v[24:27]\n"
  "v_exp_f32 v163, v163\n"
  "v_mfma_f32_16x16x32_bf16 v[28:31], a[252:255], v[128:131], v[28:31]\n"
  "ds_read_b128 a[200:203], v220 offset:9216\n"
  "v_cvt_pk_bf16_f32 v161, v162, v163\n"
  "v_mfma_f32_16x16x32_bf16 v[196:199], v[212:215], v[136:139], v[196:199]\n"
  "buffer_load_dwordx4 v224, s[12:15], s41 offen offset:2048 lds\n"
  "v_mfma_f32_16x16x32_bf16 v[32:35], a[224:227], v[136:139], v[32:35]\n"
  "ds_read_b128 a[204:207], v220 offset:9728\n"
  "v_fma_f32 v164, v164, s38, v208\n"
  "v_fma_f32 v165, v165, s38, v208\n"
  "v_mfma_f32_16x16x32_bf16 v[36:39], a[228:231], v[136:139], v[36:39]\n"
  "v_exp_f32 v164, v164\n"
  "v_mfma_f32_16x16x32_bf16 v[40:43], a[232:235], v[136:139], v[40:43]\n"
  "v_exp_f32 v165, v165\n"
  "v_mfma_f32_16x16x32_bf16 v[44:47], a[236:239], v[136:139], v[44:47]\n"
  "ds_read_b128 a[208:211], v220 offset:12288\n"
  "v_cvt_pk_bf16_f32 v162, v164, v165\n"
  "v_mfma_f32_16x16x32_bf16 v[48:51], a[240:243], v[136:139], v[48:51]\n"
  "ds_read_b128 a[212:215], v220 offset:12800\n"
  "v_fma_f32 v166, v166, s38, v208\n"
  "v_mfma_f32_16x16x32_bf16 v[52:55], a[244:247], v[136:139], v[52:55]\n"
  "buffer_load_dwordx4 v225, s[12:15], s41 offen offset:3072 lds\n"
  "v_mfma_f32_16x16x32_bf16 v[56:59], a[248:251], v[136:139], v[56:59]\n"
  "v_exp_f32 v166, v166\n"
  "v_mfma_f32_16x16x32_bf16 v[60:63], a[252:255], v[136:139], v[60:63]\n"
  "ds_read_b128 a[216:219], v220 offset:13312\n"
  "ds_read_b128 a[220:223], v220 offset:13824\n"
  "v_xor_b32 v220, s43, v220\n"
  "v_mfma_f32_16x16x32_bf16 v[200:203], v[212:215], v[144:147], v[200:203]\n"
  "v_fma_f32 v167, v167, s38, v208\n"
  "v_fma_f32 v168, v168, s38, v209\n"
  "v_mfma_f32_16x16x32_bf16 v[64:67], a[224:227], v[144:147], v[64:67]\n"
  "v_exp_f32 v167, v167\n"
  "v_mfma_f32_16x16x32_bf16 v[68:71], a[228:231], v[144:147], v[68:71]\n"
  "v_cvt_pk_bf16_f32 v163, v166, v167\n"
  "v_fma_f32 v169, v169, s38, v209\n"
  "v_mfma_f32_16x16x32_bf16 v[72:75], a[232:235], v[144:147], v[72:75]\n"
  "v_exp_f32 v168, v168\n"
  "v_mfma_f32_16x16x32_bf16 v[76:79], a[236:239], v[144:147], v[76:79]\n"
  "v_exp_f32 v169, v169\n"
  "v_mfma_f32_16x16x32_bf16 v[80:83], a[240:243], v[144:147], v[80:83]\n"
  "v_cvt_pk_bf16_f32 v168, v168, v169\n"
  "v_fma_f32 v170, v170, s38, v209\n"
  "v_mfma_f32_16x16x32_bf16 v[84:87], a[244:247], v[144:147], v[84:87]\n"
  "v_exp_f32 v170, v170\n"
  "v_mfma_f32_16x16x32_bf16 v[88:91], a[248:251], v[144:147], v[88:91]\n"
  "v_fma_f32 v171, v171, s38, v209\n"
  "v_fma_f32 v172, v172, s38, v209\n"
  "v_fma_f32 v173, v173, s38, v209\n"
  "v_mfma_f32_16x16x32_bf16 v[92:95], a[252:255], v[144:147], v[92:95]\n"
  "v_exp_f32 v171, v171\n"
  "v_mfma_f32_16x16x32_bf16 v[204:207], v[212:215], v[152:155], v[204:207]\n"
  "v_cvt_pk_bf16_f32 v169, v170, v171\n"
  "v_fma_f32 v174, v174, s38, v209\n"
  "v_mfma_f32_16x16x32_bf16 v[96:99], a[224:227], v[152:155], v[96:99]\n"
  "v_exp_f32 v172, v172\n"
  "v_mfma_f32_16x16x32_bf16 v[100:103], a[228:231], v[152:155], v[100:103]\n"
  "v_exp_f32 v173, v173\n"
  "v_mfma_f32_16x16x32_bf16 v[104:107], a[232:235], v[152:155], v[104:107]\n"
  "s_mov_b32 s41, s39\n"
  "v_cvt_pk_bf16_f32 v170, v172, v173\n"
  "v_mfma_f32_16x16x32_bf16 v[108:111], a[236:239], v[152:155], v[108:111]\n"
  "s_mov_b32 s14, s10\n"
  "v_fma_f32 v175, v175, s38, v209\n"
  "v_mfma_f32_16x16x32_bf16 v[112:115], a[240:243], v[152:155], v[112:115]\n"
  "s_add_u32 s39, s39, 0x40000\n"
  "v_exp_f32 v174, v174\n"
  "v_mfma_f32_16x16x32_bf16 v[116:119], a[244:247], v[152:155], v[116:119]\n"
  "s_xor_b32 s42, s42, s43\n"
  "v_mfma_f32_16x16x32_bf16 v[120:123], a[248:251], v[152:155], v[120:123]\n"
  "s_xor_b32 s43, s42, s43\n"
  "v_mfma_f32_16x16x32_bf16 v[124:127], a[252:255], v[152:155], v[124:127]\n"
  "s_sub_u32 s44, s44, 1\n"
  "v_exp_f32 v175, v175\n"
  "s_waitcnt vmcnt(8) lgkmcnt(0)\n"
  "v_cvt_pk_bf16_f32 v171, v174, v175\n"
  "s_cbranch_scc0 L_loop4\n"
  "L_disp:\n"
  "s_cmp_eq_u32 s48, 0\n"
  "s_cbranch_scc1 L_T\n"
  "s_cmp_eq_u32 s76, 0\n"
  "s_cbranch_scc1 L_dpoll\n"
  "L_d1:\n"
  "s_cmp_lg_u32 s47, 0\n"
  "s_cbranch_scc1 L_sw_doneD\n"
  "s_add_u32 s71, s71, 8\n"
  "s_add_u32 s58, s71, 1\n"
  "s_add_u32 s59, s71, 2\n"
  "s_add_u32 s60, s71, 3\n"
  "v_readlane_b32 s10, v228, s58\n"
  "v_readlane_b32 s39, v228, s59\n"
  "v_readlane_b32 s47, v228, s60\n"
  "s_add_u32 s58, s71, 8\n"
  "s_and_b32 s47, s47, 0xffff\n"
  "s_lshl_b32 s59, s47, 18\n"
  "s_add_u32 s59, s59, s39\n"
  "s_add_u32 s59, s59, 256\n"
  "s_min_u32 s10, s10, s59\n"
  "s_add_u32 s39, s39, s52\n"
  "s_cmp_ge_u32 s58, s55\n"
  "s_cselect_b32 s47, 0x10000, s47\n"
  "L_sw_doneD:\n"
  "s_cmp_eq_u32 s49, 0\n"
  "s_cbranch_scc1 L_dmask\n"
  "L_d2:\n"
  "s_min_u32 s58, s49, s47\n"
  "s_sub_i32 s59, s48, 3\n"
  "s_max_i32 s59, s59, 1\n"
  "s_cmp_eq_u32 s76, 0\n"
  "s_cselect_b32 s59, s59, s58\n"
  "s_min_u32 s58, s58, s59\n"
  "s_max_u32 s58, s58, 1\n"
  "s_sub_u32 s48, s48, s58\n"
  "s_sub_u32 s49, s49, s58\n"
  "s_sub_u32 s47, s47, s58\n"
  "s_sub_u32 s44, s58, 1\n"
  "s_cmp_eq_u32 s78, 4\n"
  "s_cbranch_scc1 L_loop4\n"
  "s_cmp_eq_u32 s78, 3\n"
  "s_cbranch_scc1 L_loop3\n"
  "s_cmp_eq_u32 s78, 2\n"
  "s_cbranch_scc1 L_loop2\n"
  "s_branch L_loop1\n"
  "L_dmask:\n"
  "v_lshrrev_b32 v235, 8, v226\n"
  "s_mul_i32 s58, s72, 15\n"
  "s_mov_b32 s59, 0x3f803f80\n"
  "v_mul_u32_u24 v235, 60, v235\n"
  "v_sub_u32 v235, s58, v235\n"
  "v_subrev_u32 v212, 0, v235\n"
  "v_subrev_u32 v213, 30, v235\n"
  "v_subrev_u32 v214, 240, v235\n"
  "v_subrev_u32 v215, 270, v235\n"
  "v_med3_i32 v212, v212, 0, 30\n"
  "v_med3_i32 v213, v213, 0, 30\n"
  "v_med3_i32 v214, v214, 0, 30\n"
  "v_med3_i32 v215, v215, 0, 30\n"
  "v_bfe_u32 v212, s59, 0, v212\n"
  "v_bfe_u32 v213, s59, 0, v213\n"
  "v_bfe_u32 v214, s59, 0, v214\n"
  "v_bfe_u32 v215, s59, 0, v215\n"
  "s_mov_b32 s49, 1\n"
  "s_branch L_d2\n"
  "L_dpoll:\n"
  "v_readfirstlane_b32 s62, v230\n"
  "v_readfirstlane_b32 s63, v231\n"
  "s_nop 0\n"
  "s_cmp_eq_u64 s[62:63], s[56:57]\n"
  "s_cbranch_scc0 L_d1\n"
  "s_cmp_lt_u32 s75, s87\n"
  "s_cbranch_scc1 L_dp1\n"
  "s_cmp_eq_u32 s49, 0\n"
  "s_cbranch_scc1 L_d1\n"
  "s_mov_b32 s76, 1\n"
  "s_mov_b32 s50, 2146435072\n"
  "s_min_u32 s58, s49, s47\n"
  "s_sub_i32 s59, s48, 3\n"
  "s_max_i32 s59, s59, 1\n"
  "s_cmp_eq_u32 s76, 0\n"
  "s_cselect_b32 s59, s59, s58\n"
  "s_min_u32 s58, s58, s59\n"
  "s_max_u32 s58, s58, 1\n"
  "s_sub_u32 s48, s48, s58\n"
  "s_sub_u32 s49, s49, s58\n"
  "s_sub_u32 s47, s47, s58\n"
  "s_sub_u32 s44, s58, 1\n"
  "s_branch L_loop4p\n"
  "L_dp1:\n"
  "s_add_u32 s58, s73, s81\n"
  "buffer_load_dwordx4 a[128:131], v226, s[24:27], s58 offen offset:0 sc1\n"
  "buffer_load_dwordx4 a[132:135], v226, s[24:27], s58 offen offset:1024 sc1\n"
  "buffer_load_dwordx4 a[136:139], v226, s[24:27], s58 offen offset:2048 sc1\n"
  "buffer_load_dwordx4 a[140:143], v226, s[24:27], s58 offen offset:3072 sc1\n"
  "s_add_u32 s58, s58, 4096\n"
  "buffer_load_dwordx4 a[144:147], v226, s[24:27], s58 offen offset:0 sc1\n"
  "buffer_load_dwordx4 a[148:151], v226, s[24:27], s58 offen offset:1024 sc1\n"
  "buffer_load_dwordx4 a[152:155], v226, s[24:27], s58 offen offset:2048 sc1\n"
  "buffer_load_dwordx4 a[156:159], v226, s[24:27], s58 offen offset:3072 sc1\n"
  "s_add_u32 s58, s58, 4096\n"
  "buffer_load_dwordx4 a[160:163], v226, s[24:27], s58 offen offset:0 sc1\n"
  "buffer_load_dwordx4 a[164:167], v226, s[24:27], s58 offen offset:1024 sc1\n"
  "buffer_load_dwordx4 a[168:171], v226, s[24:27], s58 offen offset:2048 sc1\n"
  "buffer_load_dwordx4 a[172:175], v226, s[24:27], s58 offen offset:3072 sc1\n"
  "s_add_u32 s58, s58, 4096\n"
  "buffer_load_dwordx4 a[176:179], v226, s[24:27], s58 offen offset:0 sc1\n"
  "buffer_load_dwordx4 a[180:183], v226, s[24:27], s58 offen offset:1024 sc1\n"
  "buffer_load_dwordx4 a[184:187], v226, s[24:27], s58 offen offset:2048 sc1\n"
  "buffer_load_dwordx4 a[188:191], v226, s[24:27], s58 offen offset:3072 sc1\n"
  "s_lshl_b32 s59, s53, 10\n"
  "s_add_u32 s58, s73, s59\n"
  "s_add_u32 s58, s58, 65536\n"
  "buffer_load_dwordx4 v[244:247], v226, s[24:27], s58 offen sc1\n"
  "s_add_u32 s73, s73, 69888\n"
  "s_add_u32 s75, s75, 1\n"
  "s_add_u32 s50, s50, 69888\n"
  "s_cmp_lt_u32 s75, s74\n"
  "s_cselect_b32 s50, s50, 2146435072\n"
  "v_mov_b32 v230, 0\n"
  "v_mov_b32 v231, 0\n"
  "s_branch L_d1\n"
  "L_T:\n"
  "s_add_u32 s70, s70, 8\n"
  "s_cmp_ge_u32 s70, s55\n"
  "s_cbranch_scc1 L_Z\n"
  "L_M:\n"
  "s_barrier\n"
  "ds_read_b64_tr_b16 a[224:225], v221 offset:0\n"
  "ds_read_b64_tr_b16 a[226:227], v221 offset:4096\n"
  "ds_read_b64_tr_b16 a[228:229], v239 offset:0\n"
  "ds_read_b64_tr_b16 a[230:231], v239 offset:4096\n"
  "ds_read_b64_tr_b16 a[232:233], v221 offset:512\n"
  "ds_read_b64_tr_b16 a[234:235], v221 offset:4608\n"
  "ds_read_b64_tr_b16 a[236:237], v239 offset:512\n"
  "ds_read_b64_tr_b16 a[238:239], v239 offset:4608\n"
  "ds_read_b64_tr_b16 a[240:241], v221 offset:1024\n"
  "ds_read_b64_tr_b16 a[242:243], v221 offset:5120\n"
  "ds_read_b64_tr_b16 a[244:245], v239 offset:1024\n"
  "ds_read_b64_tr_b16 a[246:247], v239 offset:5120\n"
  "ds_read_b64_tr_b16 a[248:249], v221 offset:1536\n"
  "ds_read_b64_tr_b16 a[250:251], v221 offset:5632\n"
  "ds_read_b64_tr_b16 a[252:253], v239 offset:1536\n"
  "ds_read_b64_tr_b16 a[254:255], v239 offset:5632\n"
  "buffer_store_dwordx2 v[232:233], v227, s[24:27], s51 offen sc1\n"
  "s_sub_u32 s58, s82, s42\n"
  "v_add_u32 v234, s58, v220\n"
  "s_cmp_ge_u32 s72, 64\n"
  "s_cbranch_scc1 L_m1M\n"
  "v_lshrrev_b32 v235, 8, v226\n"
  "s_mul_i32 s58, s72, 15\n"
  "s_mov_b32 s59, 0x3f803f80\n"
  "v_mul_u32_u24 v235, 60, v235\n"
  "v_sub_u32 v235, s58, v235\n"
  "v_subrev_u32 v216, 480, v235\n"
  "v_subrev_u32 v217, 510, v235\n"
  "v_subrev_u32 v218, 720, v235\n"
  "v_subrev_u32 v219, 750, v235\n"
  "v_med3_i32 v216, v216, 0, 30\n"
  "v_med3_i32 v217, v217, 0, 30\n"
  "v_med3_i32 v218, v218, 0, 30\n"
  "v_med3_i32 v219, v219, 0, 30\n"
  "v_bfe_u32 v216, s59, 0, v216\n"
  "v_bfe_u32 v217, s59, 0, v217\n"
  "v_bfe_u32 v218, s59, 0, v218\n"
  "v_bfe_u32 v219, s59, 0, v219\n"
  "L_m1M:\n"
  "s_cmp_lg_u32 s47, 0\n"
  "s_cbranch_scc1 L_sw_doneT1\n"
  "s_add_u32 s71, s71, 8\n"
  "s_add_u32 s58, s71, 1\n"
  "s_add_u32 s59, s71, 2\n"
  "s_add_u32 s60, s71, 3\n"
  "v_readlane_b32 s10, v228, s58\n"
  "v_readlane_b32 s39, v228, s59\n"
  "v_readlane_b32 s47, v228, s60\n"
  "s_add_u32 s58, s71, 8\n"
  "s_and_b32 s47, s47, 0xffff\n"
  "s_lshl_b32 s59, s47, 18\n"
  "s_add_u32 s59, s59, s39\n"
  "s_add_u32 s59, s59, 256\n"
  "s_min_u32 s10, s10, s59\n"
  "s_add_u32 s39, s39, s52\n"
  "s_cmp_ge_u32 s58, s55\n"
  "s_cselect_b32 s47, 0x10000, s47\n"
  "L_sw_doneT1:\n"
  "s_sub_u32 s47, s47, 1\n"
  "s_waitcnt lgkmcnt(0)\n"
  "v_mfma_f32_16x16x32_bf16 v[192:195], v[216:219], v[160:163], v[192:195]\n"
  "ds_read_b128 a[0:3], v234 offset:0\n"
  "ds_read_b128 a[4:7], v234 offset:512\n"
  "v_mfma_f32_16x16x32_bf16 v[0:3], a[224:227], v[160:163], v[0:3]\n"
  "ds_read_b128 a[8:11], v234 offset:1024\n"
  "ds_read_b128 a[12:15], v234 offset:1536\n"
  "v_fma_f32 v176, v176, s38, v210\n"
  "v_mfma_f32_16x16x32_bf16 v[4:7], a[228:231], v[160:163], v[4:7]\n"
  "v_exp_f32 v176, v176\n"
  "v_mfma_f32_16x16x32_bf16 v[8:11], a[232:235], v[160:163], v[8:11]\n"
  "ds_read_b128 a[16:19], v234 offset:4096\n"
  "ds_read_b128 a[20:23], v234 offset:4608\n"
  "v_fma_f32 v177, v177, s38, v210\n"
  "v_mfma_f32_16x16x32_bf16 v[12:15], a[236:239], v[160:163], v[12:15]\n"
  "v_exp_f32 v177, v177\n"
  "v_mfma_f32_16x16x32_bf16 v[16:19], a[240:243], v[160:163], v[16:19]\n"
  "ds_read_b128 a[24:27], v234 offset:5120\n"
  "ds_read_b128 a[28:31], v234 offset:5632\n"
  "v_cvt_pk_bf16_f32 v176, v176, v177\n"
  "v_mfma_f32_16x16x32_bf16 v[20:23], a[244:247], v[160:163], v[20:23]\n"
  "ds_read_b128 a[32:35], v234 offset:8192\n"
  "ds_read_b128 a[36:39], v234 offset:8704\n"
  "v_mfma_f32_16x16x32_bf16 v[24:27], a[248:251], v[160:163], v[24:27]\n"
  "ds_read_b128 a[40:43], v234 offset:9216\n"
  "v_fma_f32 v178, v178, s38, v210\n"
  "v_fma_f32 v179, v179, s38, v210\n"
  "v_mfma_f32_16x16x32_bf16 v[28:31], a[252:255], v[160:163], v[28:31]\n"
  "v_exp_f32 v178, v178\n"
  "v_mfma_f32_16x16x32_bf16 v[196:199], v[216:219], v[168:171], v[196:199]\n"
  "v_exp_f32 v179, v179\n"
  "v_mfma_f32_16x16x32_bf16 v[32:35], a[224:227], v[168:171], v[32:35]\n"
  "ds_read_b128 a[44:47], v234 offset:9728\n"
  "ds_read_b128 a[48:51], v234 offset:12288\n"
  "v_cvt_pk_bf16_f32 v177, v178, v179\n"
  "v_mfma_f32_16x16x32_bf16 v[36:39], a[228:231], v[168:171], v[36:39]\n"
  "ds_read_b128 a[52:55], v234 offset:12800\n"
  "ds_read_b128 a[56:59], v234 offset:13312\n"
  "v_mfma_f32_16x16x32_bf16 v[40:43], a[232:235], v[168:171], v[40:43]\n"
  "ds_read_b128 a[60:63], v234 offset:13824\n"
  "v_fma_f32 v180, v180, s38, v210\n"
  "v_fma_f32 v181, v181, s38, v210\n"
  "v_mfma_f32_16x16x32_bf16 v[44:47], a[236:239], v[168:171], v[44:47]\n"
  "v_exp_f32 v180, v180\n"
  "v_mfma_f32_16x16x32_bf16 v[48:51], a[240:243], v[168:171], v[48:51]\n"
  "v_exp_f32 v181, v181\n"
  "v_fma_f32 v182, v182, s38, v210\n"
  "v_mfma_f32_16x16x32_bf16 v[52:55], a[244:247], v[168:171], v[52:55]\n"
  "v_cvt_pk_bf16_f32 v178, v180, v181\n"
  "v_fma_f32 v183, v183, s38, v210\n"
  "v_mfma_f32_16x16x32_bf16 v[56:59], a[248:251], v[168:171], v[56:59]\n"
  "v_exp_f32 v182, v182\n"
  "v_mfma_f32_16x16x32_bf16 v[60:63], a[252:255], v[168:171], v[60:63]\n"
  "v_exp_f32 v183, v183\n"
  "s_waitcnt lgkmcnt(0)\n"
  "v_cvt_pk_bf16_f32 v179, v182, v183\n"
  "s_cmp_lg_u32 s77, -1\n"
  "s_cbranch_scc1 L_Hp\n"
  "v_mfma_f32_16x16x32_bf16 v[128:131], a[192:195], a[0:3], 0\n"
  "v_and_b32 v248, 0xf0, v226\n"
  "v_fma_f32 v184, v184, s38, v211\n"
  "v_mfma_f32_16x16x32_bf16 v[128:131], a[196:199], a[4:7], v[128:131]\n"
  "v_exp_f32 v184, v184\n"
  "v_mfma_f32_16x16x32_bf16 v[128:131], a[200:203], a[8:11], v[128:131]\n"
  "v_and_b32 v254, 0x70, v226\n"
  "v_fma_f32 v185, v185, s38, v211\n"
  "v_mfma_f32_16x16x32_bf16 v[128:131], a[204:207], a[12:15], v[128:131]\n"
  "v_exp_f32 v185, v185\n"
  "v_mfma_f32_16x16x32_bf16 v[132:135], a[208:211], a[0:3], 0\n"
  "v_lshl_add_u32 v248, v248, 4, v254\n"
  "v_cvt_pk_bf16_f32 v184, v184, v185\n"
  "v_mfma_f32_16x16x32_bf16 v[132:135], a[212:215], a[4:7], v[132:135]\n"
  "v_lshrrev_b32 v254, 5, v226\n"
  "v_fma_f32 v186, v186, s38, v211\n"
  "v_mfma_f32_16x16x32_bf16 v[132:135], a[216:219], a[8:11], v[132:135]\n"
  "v_and_b32 v254, 0x18, v254\n"
  "v_xor_b32 v248, v248, v254\n"
  "v_mfma_f32_16x16x32_bf16 v[132:135], a[220:223], a[12:15], v[132:135]\n"
  "v_exp_f32 v186, v186\n"
  "v_mfma_f32_16x16x32_bf16 v[136:139], a[192:195], a[16:19], 0\n"
  "v_add_u32 v248, s82, v248\n"
  "v_fma_f32 v187, v187, s38, v211\n"
  "v_mfma_f32_16x16x32_bf16 v[136:139], a[196:199], a[20:23], v[136:139]\n"
  "v_xor_b32 v249, 32, v248\n"
  "v_xor_b32 v250, 64, v248\n"
  "v_mfma_f32_16x16x32_bf16 v[136:139], a[200:203], a[24:27], v[136:139]\n"
  "v_xor_b32 v251, 96, v248\n"
  "v_lshrrev_b32 v254, 4, v226\n"
  "v_mfma_f32_16x16x32_bf16 v[136:139], a[204:207], a[28:31], v[136:139]\n"
  "v_exp_f32 v187, v187\n"
  "v_mfma_f32_16x16x32_bf16 v[140:143], a[208:211], a[16:19], 0\n"
  "v_and_b32 v254, 0x30, v254\n"
  "v_cvt_pk_bf16_f32 v185, v186, v187\n"
  "v_mfma_f32_16x16x32_bf16 v[140:143], a[212:215], a[20:23], v[140:143]\n"
  "v_xor_b32 v252, v226, v254\n"
  "v_fma_f32 v188, v188, s38, v211\n"
  "v_mfma_f32_16x16x32_bf16 v[140:143], a[216:219], a[24:27], v[140:143]\n"
  "v_add_u32 v252, s82, v252\n"
  "v_xor_b32 v253, 64, v252\n"
  "v_mfma_f32_16x16x32_bf16 v[140:143], a[220:223], a[28:31], v[140:143]\n"
  "v_exp_f32 v188, v188\n"
  "v_mfma_f32_16x16x32_bf16 v[144:147], a[192:195], a[32:35], 0\n"
  "s_mov_b32 s88, 0x35800000\n"
  "v_fma_f32 v189, v189, s38, v211\n"
  "v_mfma_f32_16x16x32_bf16 v[144:147], a[196:199], a[36:39], v[144:147]\n"
  "v_mov_b32 v234, 0x67800000\n"
  "v_med3_f32 v193, v192, s88, v234\n"
  "v_mfma_f32_16x16x32_bf16 v[144:147], a[200:203], a[40:43], v[144:147]\n"
  "v_rcp_f32 v194, v192\n"
  "v_mfma_f32_16x16x32_bf16 v[144:147], a[204:207], a[44:47], v[144:147]\n"
  "v_exp_f32 v189, v189\n"
  "v_mfma_f32_16x16x32_bf16 v[148:151], a[208:211], a[32:35], 0\n"
  "v_cmp_neq_f32 s[62:63], v193, v192\n"
  "v_cvt_pk_bf16_f32 v186, v188, v189\n"
  "v_mfma_f32_16x16x32_bf16 v[148:151], a[212:215], a[36:39], v[148:151]\n"
  "v_med3_f32 v197, v196, s88, v234\n"
  "v_fma_f32 v190, v190, s38, v211\n"
  "v_mfma_f32_16x16x32_bf16 v[148:151], a[216:219], a[40:43], v[148:151]\n"
  "v_rcp_f32 v198, v196\n"
  "v_mfma_f32_16x16x32_bf16 v[148:151], a[220:223], a[44:47], v[148:151]\n"
  "v_exp_f32 v190, v190\n"
  "v_mfma_f32_16x16x32_bf16 v[152:155], a[192:195], a[48:51], 0\n"
  "v_cmp_neq_f32 s[64:65], v197, v196\n"
  "v_fma_f32 v191, v191, s38, v211\n"
  "v_mfma_f32_16x16x32_bf16 v[152:155], a[196:199], a[52:55], v[152:155]\n"
  "v_mov_b32 v212, 0x3f803f80\n"
  "v_mov_b32 v213, 0x3f803f80\n"
  "v_mfma_f32_16x16x32_bf16 v[152:155], a[200:203], a[56:59], v[152:155]\n"
  "v_mov_b32 v214, 0x3f803f80\n"
  "v_mov_b32 v215, 0x3f803f80\n"
  "v_mfma_f32_16x16x32_bf16 v[152:155], a[204:207], a[60:63], v[152:155]\n"
  "v_exp_f32 v191, v191\n"
  "v_mfma_f32_16x16x32_bf16 v[156:159], a[208:211], a[48:51], 0\n"
  "v_cvt_pk_bf16_f32 v187, v190, v191\n"
  "v_mfma_f32_16x16x32_bf16 v[156:159], a[212:215], a[52:55], v[156:159]\n"
  "v_mfma_f32_16x16x32_bf16 v[156:159], a[216:219], a[56:59], v[156:159]\n"
  "v_mfma_f32_16x16x32_bf16 v[156:159], a[220:223], a[60:63], v[156:159]\n"
  "v_mfma_f32_16x16x32_bf16 v[200:203], v[216:219], v[176:179], v[200:203]\n"
  "ds_read_b128 a[192:195], v220 offset:0\n"
  "v_max3_f32 v240, v128, v129, v130\n"
  "v_mfma_f32_16x16x32_bf16 v[64:67], a[224:227], v[176:179], v[64:67]\n"
  "v_max3_f32 v242, v136, v137, v138\n"
  "v_max3_f32 v244, v144, v145, v146\n"
  "v_mfma_f32_16x16x32_bf16 v[68:71], a[228:231], v[176:179], v[68:71]\n"
  "ds_read_b128 a[196:199], v220 offset:512\n"
  "v_max3_f32 v246, v152, v153, v154\n"
  "v_mfma_f32_16x16x32_bf16 v[72:75], a[232:235], v[176:179], v[72:75]\n"
  "v_max_f32 v240, v240, v131\n"
  "v_max_f32 v242, v242, v139\n"
  "v_mfma_f32_16x16x32_bf16 v[76:79], a[236:239], v[176:179], v[76:79]\n"
  "ds_read_b128 a[200:203], v220 offset:1024\n"
  "v_max_f32 v244, v244, v147\n"
  "v_mfma_f32_16x16x32_bf16 v[80:83], a[240:243], v[176:179], v[80:83]\n"
  "v_max_f32 v246, v246, v155\n"
  "v_mov_b32 v241, v240\n"
  "v_mfma_f32_16x16x32_bf16 v[84:87], a[244:247], v[176:179], v[84:87]\n"
  "ds_read_b128 a[204:207], v220 offset:1536\n"
  "v_mov_b32 v243, v242\n"
  "v_mfma_f32_16x16x32_bf16 v[88:91], a[248:251], v[176:179], v[88:91]\n"
  "v_mov_b32 v245, v244\n"
  "v_mov_b32 v247, v246\n"
  "v_mfma_f32_16x16x32_bf16 v[92:95], a[252:255], v[176:179], v[92:95]\n"
  "ds_read_b128 a[208:211], v220 offset:4096\n"
  "v_mfma_f32_16x16x32_bf16 v[204:207], v[216:219], v[184:187], v[204:207]\n"
  "v_permlane32_swap_b32 v241, v240\n"
  "v_mfma_f32_16x16x32_bf16 v[96:99], a[224:227], v[184:187], v[96:99]\n"
  "ds_read_b128 a[212:215], v220 offset:4608\n"
  "v_mfma_f32_16x16x32_bf16 v[100:103], a[228:231], v[184:187], v[100:103]\n"
  "v_permlane32_swap_b32 v243, v242\n"
  "v_mfma_f32_16x16x32_bf16 v[104:107], a[232:235], v[184:187], v[104:107]\n"
  "ds_read_b128 a[216:219], v220 offset:5120\n"
  "v_mfma_f32_16x16x32_bf16 v[108:111], a[236:239], v[184:187], v[108:111]\n"
  "v_permlane32_swap_b32 v245, v244\n"
  "v_mfma_f32_16x16x32_bf16 v[112:115], a[240:243], v[184:187], v[112:115]\n"
  "ds_read_b128 a[220:223], v220 offset:5632\n"
  "v_mfma_f32_16x16x32_bf16 v[116:119], a[244:247], v[184:187], v[116:119]\n"
  "v_permlane32_swap_b32 v247, v246\n"
  "v_mfma_f32_16x16x32_bf16 v[120:123], a[248:251], v[184:187], v[120:123]\n"
  "v_max_f32 v240, v240, v241\n"
  "v_max_f32 v242, v242, v243\n"
  "v_mfma_f32_16x16x32_bf16 v[124:127], a[252:255], v[184:187], v[124:127]\n"
  "v_max_f32 v244, v244, v245\n"
  "v_max_f32 v246, v246, v247\n"
  "v_mov_b32 v241, v240\n"
  "v_mov_b32 v243, v242\n"
  "v_mov_b32 v245, v244\n"
  "v_mov_b32 v247, v246\n"
  "v_permlane16_swap_b32 v241, v240\n"
  "v_permlane16_swap_b32 v243, v242\n"
  "v_permlane16_swap_b32 v245, v244\n"
  "v_permlane16_swap_b32 v247, v246\n"
  "v_max_f32 v240, v240, v241\n"
  "v_max_f32 v242, v242, v243\n"
  "v_max_f32 v244, v244, v245\n"
  "v_max_f32 v246, v246, v247\n"
  "v_mul_f32 v208, -v240, s38\n"
  "v_mul_f32 v209, -v242, s38\n"
  "v_mul_f32 v210, -v244, s38\n"
  "v_mul_f32 v211, -v246, s38\n"
  "v_mov_b32 v216, 0x3f803f80\n"
  "v_mov_b32 v217, 0x3f803f80\n"
  "v_mov_b32 v218, 0x3f803f80\n"
  "v_mov_b32 v219, 0x3f803f80\n"
  "v_med3_f32 v201, v200, s88, v234\n"
  "v_rcp_f32 v202, v200\n"
  "v_cmp_neq_f32 s[66:67], v201, v200\n"
  "v_med3_f32 v205, v204, s88, v234\n"
  "v_rcp_f32 v206, v204\n"
  "v_cmp_neq_f32 s[68:69], v205, v204\n"
  "s_cmp_ge_u32 s35, 2146435072\n"
  "s_cselect_b64 s[64:65], 0, s[64:65]\n"
  "s_or_b64 s[62:63], s[62:63], s[64:65]\n"
  "s_cmp_ge_u32 s36, 2146435072\n"
  "s_cselect_b64 s[66:67], 0, s[66:67]\n"
  "s_or_b64 s[62:63], s[62:63], s[66:67]\n"
  "s_cmp_ge_u32 s37, 2146435072\n"
  "s_cselect_b64 s[68:69], 0, s[68:69]\n"
  "s_or_b64 s[62:63], s[62:63], s[68:69]\n"
  "s_cselect_b32 s58, 1, 0\n"
  "s_lshl_b32 s58, s58, s80\n"
  "s_or_b32 s54, s54, s58\n"
  "v_pk_mul_f32 v[0:1], v[0:1], v[194:195] op_sel_hi:[1,0]\n"
  "v_pk_mul_f32 v[2:3], v[2:3], v[194:195] op_sel_hi:[1,0]\n"
  "v_pk_mul_f32 v[4:5], v[4:5], v[194:195] op_sel_hi:[1,0]\n"
  "v_pk_mul_f32 v[6:7], v[6:7], v[194:195] op_sel_hi:[1,0]\n"
  "v_pk_mul_f32 v[8:9], v[8:9], v[194:195] op_sel_hi:[1,0]\n"
  "v_pk_mul_f32 v[10:11], v[10:11], v[194:195] op_sel_hi:[1,0]\n"
  "v_pk_mul_f32 v[12:13], v[12:13], v[194:195] op_sel_hi:[1,0]\n"
  "v_pk_mul_f32 v[14:15], v[14:15], v[194:195] op_sel_hi:[1,0]\n"
  "v_pk_mul_f32 v[16:17], v[16:17], v[194:195] op_sel_hi:[1,0]\n"
  "v_pk_mul_f32 v[18:19], v[18:19], v[194:195] op_sel_hi:[1,0]\n"
  "v_pk_mul_f32 v[20:21], v[20:21], v[194:195] op_sel_hi:[1,0]\n"
  "v_pk_mul_f32 v[22:23], v[22:23], v[194:195] op_sel_hi:[1,0]\n"
  "v_pk_mul_f32 v[24:25], v[24:25], v[194:195] op_sel_hi:[1,0]\n"
  "v_pk_mul_f32 v[26:27], v[26:27], v[194:195] op_sel_hi:[1,0]\n"
  "v_pk_mul_f32 v[28:29], v[28:29], v[194:195] op_sel_hi:[1,0]\n"
  "v_pk_mul_f32 v[30:31], v[30:31], v[194:195] op_sel_hi:[1,0]\n"
  "v_cvt_pk_bf16_f32 v0, v0, v1\n"
  "v_cvt_pk_bf16_f32 v1, v2, v3\n"
  "v_cvt_pk_bf16_f32 v4, v4, v5\n"
  "v_cvt_pk_bf16_f32 v5, v6, v7\n"
  "v_cvt_pk_bf16_f32 v8, v8, v9\n"
  "v_cvt_pk_bf16_f32 v9, v10, v11\n"
  "v_cvt_pk_bf16_f32 v12, v12, v13\n"
  "v_cvt_pk_bf16_f32 v13, v14, v15\n"
  "v_cvt_pk_bf16_f32 v16, v16, v17\n"
  "v_cvt_pk_bf16_f32 v17, v18, v19\n"
  "v_cvt_pk_bf16_f32 v20, v20, v21\n"
  "v_cvt_pk_bf16_f32 v21, v22, v23\n"
  "v_cvt_pk_bf16_f32 v24, v24, v25\n"
  "v_cvt_pk_bf16_f32 v25, v26, v27\n"
  "v_cvt_pk_bf16_f32 v28, v28, v29\n"
  "v_cvt_pk_bf16_f32 v29, v30, v31\n"
  "ds_write_b64 v248, v[0:1] offset:0\n"
  "ds_write_b64 v249, v[4:5] offset:0\n"
  "ds_write_b64 v250, v[8:9] offset:0\n"
  "ds_write_b64 v251, v[12:13] offset:0\n"
  "ds_write_b64 v248, v[16:17] offset:128\n"
  "ds_write_b64 v249, v[20:21] offset:128\n"
  "ds_write_b64 v250, v[24:25] offset:128\n"
  "ds_write_b64 v251, v[28:29] offset:128\n"
  "s_cmp_eq_u32 s78, 1\n"
  "s_cbranch_scc1 L_Me_x1\n"
  "v_pk_mul_f32 v[64:65], v[64:65], v[202:203] op_sel_hi:[1,0]\n"
  "v_pk_mul_f32 v[66:67], v[66:67], v[202:203] op_sel_hi:[1,0]\n"
  "v_pk_mul_f32 v[68:69], v[68:69], v[202:203] op_sel_hi:[1,0]\n"
  "v_pk_mul_f32 v[70:71], v[70:71], v[202:203] op_sel_hi:[1,0]\n"
  "v_pk_mul_f32 v[72:73], v[72:73], v[202:203] op_sel_hi:[1,0]\n"
  "v_pk_mul_f32 v[74:75], v[74:75], v[202:203] op_sel_hi:[1,0]\n"
  "v_pk_mul_f32 v[76:77], v[76:77], v[202:203] op_sel_hi:[1,0]\n"
  "v_pk_mul_f32 v[78:79], v[78:79], v[202:203] op_sel_hi:[1,0]\n"
  "v_pk_mul_f32 v[80:81], v[80:81], v[202:203] op_sel_hi:[1,0]\n"
  "v_pk_mul_f32 v[82:83], v[82:83], v[202:203] op_sel_hi:[1,0]\n"
  "v_pk_mul_f32 v[84:85], v[84:85], v[202:203] op_sel_hi:[1,0]\n"
  "v_pk_mul_f32 v[86:87], v[86:87], v[202:203] op_sel_hi:[1,0]\n"
  "v_pk_mul_f32 v[88:89], v[88:89], v[202:203] op_sel_hi:[1,0]\n"
  "v_pk_mul_f32 v[90:91], v[90:91], v[202:203] op_sel_hi:[1,0]\n"
  "v_pk_mul_f32 v[92:93], v[92:93], v[202:203] op_sel_hi:[1,0]\n"
  "v_pk_mul_f32 v[94:95], v[94:95], v[202:203] op_sel_hi:[1,0]\n"
  "s_waitcnt lgkmcnt(0)\n"
  "ds_read_b128 v[0:3], v252 offset:0\n"
  "ds_read_b128 v[4:7], v253 offset:1024\n"
  "ds_read_b128 v[8:11], v252 offset:2048\n"
  "ds_read_b128 v[12:15], v253 offset:3072\n"
  "v_cvt_pk_bf16_f32 v64, v64, v65\n"
  "v_cvt_pk_bf16_f32 v65, v66, v67\n"
  "v_cvt_pk_bf16_f32 v68, v68, v69\n"
  "v_cvt_pk_bf16_f32 v69, v70, v71\n"
  "v_cvt_pk_bf16_f32 v72, v72, v73\n"
  "v_cvt_pk_bf16_f32 v73, v74, v75\n"
  "v_cvt_pk_bf16_f32 v76, v76, v77\n"
  "v_cvt_pk_bf16_f32 v77, v78, v79\n"
  "v_cvt_pk_bf16_f32 v80, v80, v81\n"
  "v_cvt_pk_bf16_f32 v81, v82, v83\n"
  "v_cvt_pk_bf16_f32 v84, v84, v85\n"
  "v_cvt_pk_bf16_f32 v85, v86, v87\n"
  "v_cvt_pk_bf16_f32 v88, v88, v89\n"
  "v_cvt_pk_bf16_f32 v89, v90, v91\n"
  "v_cvt_pk_bf16_f32 v92, v92, v93\n"
  "v_cvt_pk_bf16_f32 v93, v94, v95\n"
  "ds_write_b64 v248, v[64:65] offset:8192\n"
  "ds_write_b64 v249, v[68:69] offset:8192\n"
  "ds_write_b64 v250, v[72:73] offset:8192\n"
  "ds_write_b64 v251, v[76:77] offset:8192\n"
  "ds_write_b64 v248, v[80:81] offset:8320\n"
  "ds_write_b64 v249, v[84:85] offset:8320\n"
  "ds_write_b64 v250, v[88:89] offset:8320\n"
  "ds_write_b64 v251, v[92:93] offset:8320\n"
  "s_cmp_eq_u32 s78, 2\n"
  "s_cbranch_scc1 L_Me_x2\n"
  "v_pk_mul_f32 v[32:33], v[32:33], v[198:199] op_sel_hi:[1,0]\n"
  "v_pk_mul_f32 v[34:35], v[34:35], v[198:199] op_sel_hi:[1,0]\n"
  "v_pk_mul_f32 v[36:37], v[36:37], v[198:199] op_sel_hi:[1,0]\n"
  "v_pk_mul_f32 v[38:39], v[38:39], v[198:199] op_sel_hi:[1,0]\n"
  "v_pk_mul_f32 v[40:41], v[40:41], v[198:199] op_sel_hi:[1,0]\n"
  "v_pk_mul_f32 v[42:43], v[42:43], v[198:199] op_sel_hi:[1,0]\n"
  "v_pk_mul_f32 v[44:45], v[44:45], v[198:199] op_sel_hi:[1,0]\n"
  "v_pk_mul_f32 v[46:47], v[46:47], v[198:199] op_sel_hi:[1,0]\n"
  "v_pk_mul_f32 v[48:49], v[48:49], v[198:199] op_sel_hi:[1,0]\n"
  "v_pk_mul_f32 v[50:51], v[50:51], v[198:199] op_sel_hi:[1,0]\n"
  "v_pk_mul_f32 v[52:53], v[52:53], v[198:199] op_sel_hi:[1,0]\n"
  "v_pk_mul_f32 v[54:55], v[54:55], v[198:199] op_sel_hi:[1,0]\n"
  "v_pk_mul_f32 v[56:57], v[56:57], v[198:199] op_sel_hi:[1,0]\n"
  "v_pk_mul_f32 v[58:59], v[58:59], v[198:199] op_sel_hi:[1,0]\n"
  "v_pk_mul_f32 v[60:61], v[60:61], v[198:199] op_sel_hi:[1,0]\n"
  "v_pk_mul_f32 v[62:63], v[62:63], v[198:199] op_sel_hi:[1,0]\n"
  "s_waitcnt lgkmcnt(0)\n"
  "ds_read_b128 v[64:67], v252 offset:8192\n"
  "ds_read_b128 v[68:71], v253 offset:9216\n"
  "ds_read_b128 v[72:75], v252 offset:10240\n"
  "ds_read_b128 v[76:79], v253 offset:11264\n"
  "v_cvt_pk_bf16_f32 v32, v32, v33\n"
  "v_cvt_pk_bf16_f32 v33, v34, v35\n"
  "v_cvt_pk_bf16_f32 v36, v36, v37\n"
  "v_cvt_pk_bf16_f32 v37, v38, v39\n"
  "v_cvt_pk_bf16_f32 v40, v40, v41\n"
  "v_cvt_pk_bf16_f32 v41, v42, v43\n"
  "v_cvt_pk_bf16_f32 v44, v44, v45\n"
  "v_cvt_pk_bf16_f32 v45, v46, v47\n"
  "v_cvt_pk_bf16_f32 v48, v48, v49\n"
  "v_cvt_pk_bf16_f32 v49, v50, v51\n"
  "v_cvt_pk_bf16_f32 v52, v52, v53\n"
  "v_cvt_pk_bf16_f32 v53, v54, v55\n"
  "v_cvt_pk_bf16_f32 v56, v56, v57\n"
  "v_cvt_pk_bf16_f32 v57, v58, v59\n"
  "v_cvt_pk_bf16_f32 v60, v60, v61\n"
  "v_cvt_pk_bf16_f32 v61, v62, v63\n"
  "ds_write_b64 v248, v[32:33] offset:4096\n"
  "ds_write_b64 v249, v[36:37] offset:4096\n"
  "ds_write_b64 v250, v[40:41] offset:4096\n"
  "ds_write_b64 v251, v[44:45] offset:4096\n"
  "ds_write_b64 v248, v[48:49] offset:4224\n"
  "ds_write_b64 v249, v[52:53] offset:4224\n"
  "ds_write_b64 v250, v[56:57] offset:4224\n"
  "ds_write_b64 v251, v[60:61] offset:4224\n"
  "s_cmp_eq_u32 s78, 3\n"
  "s_cbranch_scc1 L_Me_x3\n"
  "v_pk_mul_f32 v[96:97], v[96:97], v[206:207] op_sel_hi:[1,0]\n"
  "v_pk_mul_f32 v[98:99], v[98:99], v[206:207] op_sel_hi:[1,0]\n"
  "v_pk_mul_f32 v[100:101], v[100:101], v[206:207] op_sel_hi:[1,0]\n"
  "v_pk_mul_f32 v[102:103], v[102:103], v[206:207] op_sel_hi:[1,0]\n"
  "v_pk_mul_f32 v[104:105], v[104:105], v[206:207] op_sel_hi:[1,0]\n"
  "v_pk_mul_f32 v[106:107], v[106:107], v[206:207] op_sel_hi:[1,0]\n"
  "v_pk_mul_f32 v[108:109], v[108:109], v[206:207] op_sel_hi:[1,0]\n"
  "v_pk_mul_f32 v[110:111], v[110:111], v[206:207] op_sel_hi:[1,0]\n"
  "v_pk_mul_f32 v[112:113], v[112:113], v[206:207] op_sel_hi:[1,0]\n"
  "v_pk_mul_f32 v[114:115], v[114:115], v[206:207] op_sel_hi:[1,0]\n"
  "v_pk_mul_f32 v[116:117], v[116:117], v[206:207] op_sel_hi:[1,0]\n"
  "v_pk_mul_f32 v[118:119], v[118:119], v[206:207] op_sel_hi:[1,0]\n"
  "v_pk_mul_f32 v[120:121], v[120:121], v[206:207] op_sel_hi:[1,0]\n"
  "v_pk_mul_f32 v[122:123], v[122:123], v[206:207] op_sel_hi:[1,0]\n"
  "v_pk_mul_f32 v[124:125], v[124:125], v[206:207] op_sel_hi:[1,0]\n"
  "v_pk_mul_f32 v[126:127], v[126:127], v[206:207] op_sel_hi:[1,0]\n"
  "s_waitcnt lgkmcnt(0)\n"
  "ds_read_b128 v[32:35], v252 offset:4096\n"
  "ds_read_b128 v[36:39], v253 offset:5120\n"
  "ds_read_b128 v[40:43], v252 offset:6144\n"
  "ds_read_b128 v[44:47], v253 offset:7168\n"
  "v_cvt_pk_bf16_f32 v96, v96, v97\n"
  "v_cvt_pk_bf16_f32 v97, v98, v99\n"
  "v_cvt_pk_bf16_f32 v100, v100, v101\n"
  "v_cvt_pk_bf16_f32 v101, v102, v103\n"
  "v_cvt_pk_bf16_f32 v104, v104, v105\n"
  "v_cvt_pk_bf16_f32 v105, v106, v107\n"
  "v_cvt_pk_bf16_f32 v108, v108, v109\n"
  "v_cvt_pk_bf16_f32 v109, v110, v111\n"
  "v_cvt_pk_bf16_f32 v112, v112, v113\n"
  "v_cvt_pk_bf16_f32 v113, v114, v115\n"
  "v_cvt_pk_bf16_f32 v116, v116, v117\n"
  "v_cvt_pk_bf16_f32 v117, v118, v119\n"
  "v_cvt_pk_bf16_f32 v120, v120, v121\n"
  "v_cvt_pk_bf16_f32 v121, v122, v123\n"
  "v_cvt_pk_bf16_f32 v124, v124, v125\n"
  "v_cvt_pk_bf16_f32 v125, v126, v127\n"
  "ds_write_b64 v248, v[96:97] offset:12288\n"
  "ds_write_b64 v249, v[100:101] offset:12288\n"
  "ds_write_b64 v250, v[104:105] offset:12288\n"
  "ds_write_b64 v251, v[108:109] offset:12288\n"
  "ds_write_b64 v248, v[112:113] offset:12416\n"
  "ds_write_b64 v249, v[116:117] offset:12416\n"
  "ds_write_b64 v250, v[120:121] offset:12416\n"
  "ds_write_b64 v251, v[124:125] offset:12416\n"
  "s_waitcnt lgkmcnt(0)\n"
  "ds_read_b128 v[96:99], v252 offset:12288\n"
  "ds_read_b128 v[100:103], v253 offset:13312\n"
  "ds_read_b128 v[104:107], v252 offset:14336\n"
  "ds_read_b128 v[108:111], v253 offset:15360\n"
  "s_branch L_Hs\n"
  "L_Me_x1:\n"
  "s_waitcnt lgkmcnt(0)\n"
  "ds_read_b128 v[0:3], v252 offset:0\n"
  "ds_read_b128 v[4:7], v253 offset:1024\n"
  "ds_read_b128 v[8:11], v252 offset:2048\n"
  "ds_read_b128 v[12:15], v253 offset:3072\n"
  "s_branch L_Hs\n"
  "L_Me_x2:\n"
  "s_waitcnt lgkmcnt(0)\n"
  "ds_read_b128 v[64:67], v252 offset:8192\n"
  "ds_read_b128 v[68:71], v253 offset:9216\n"
  "ds_read_b128 v[72:75], v252 offset:10240\n"
  "ds_read_b128 v[76:79], v253 offset:11264\n"
  "s_branch L_Hs\n"
  "L_Me_x3:\n"
  "s_waitcnt lgkmcnt(0)\n"
  "ds_read_b128 v[32:35], v252 offset:4096\n"
  "ds_read_b128 v[36:39], v253 offset:5120\n"
  "ds_read_b128 v[40:43], v252 offset:6144\n"
  "ds_read_b128 v[44:47], v253 offset:7168\n"
  "s_branch L_Hs\n"
  "L_Hs:\n"
  "s_waitcnt lgkmcnt(0)\n"
  "v_fma_f32 v128, v128, s38, v208\n"
  "v_fma_f32 v129, v129, s38, v208\n"
  "buffer_store_dwordx4 v[0:3], v229, s[20:23], s34 offen sc1 nt\n"
  "v_exp_f32 v128, v128\n"
  "v_fma_f32 v130, v130, s38, v208\n"
  "s_add_u32 s34, s34, 0x4000\n"
  "buffer_store_dwordx4 v[4:7], v229, s[20:23], s34 offen sc1 nt\n"
  "v_exp_f32 v129, v129\n"
  "v_fma_f32 v131, v131, s38, v208\n"
  "s_add_u32 s34, s34, 0x4000\n"
  "buffer_store_dwordx4 v[8:11], v229, s[20:23], s34 offen sc1 nt\n"
  "v_exp_f32 v130, v130\n"
  "v_cvt_pk_bf16_f32 v128, v128, v129\n"
  "s_add_u32 s34, s34, 0x4000\n"
  "buffer_store_dwordx4 v[12:15], v229, s[20:23], s34 offen sc1 nt\n"
  "v_fma_f32 v132, v132, s38, v208\n"
  "v_exp_f32 v131, v131\n"
  "v_fma_f32 v133, v133, s38, v208\n"
  "v_exp_f32 v132, v132\n"
  "buffer_store_dwordx4 v[64:67], v229, s[20:23], s36 offen sc1 nt\n"
  "v_cvt_pk_bf16_f32 v129, v130, v131\n"
  "v_fma_f32 v134, v134, s38, v208\n"
  "s_add_u32 s36, s36, 0x4000\n"
  "buffer_store_dwordx4 v[68:71], v229, s[20:23], s36 offen sc1 nt\n"
  "v_exp_f32 v133, v133\n"
  "v_fma_f32 v135, v135, s38, v208\n"
  "s_add_u32 s36, s36, 0x4000\n"
  "buffer_store_dwordx4 v[72:75], v229, s[20:23], s36 offen sc1 nt\n"
  "v_exp_f32 v134, v134\n"
  "v_cvt_pk_bf16_f32 v130, v132, v133\n"
  "s_add_u32 s36, s36, 0x4000\n"
  "buffer_store_dwordx4 v[76:79], v229, s[20:23], s36 offen sc1 nt\n"
  "v_fma_f32 v136, v136, s38, v209\n"
  "v_exp_f32 v135, v135\n"
  "v_mfma_f32_32x32x8_bf16 v[0:15], v[236:237], v[236:237], 0\n"
  "v_mfma_f32_32x32x8_bf16 v[16:31], v[236:237], v[236:237], 0\n"
  "v_fma_f32 v137, v137, s38, v209\n"
  "v_exp_f32 v136, v136\n"
  "buffer_store_dwordx4 v[32:35], v229, s[20:23], s35 offen sc1 nt\n"
  "v_cvt_pk_bf16_f32 v131, v134, v135\n"
  "v_fma_f32 v138, v138, s38, v209\n"
  "s_add_u32 s35, s35, 0x4000\n"
  "buffer_store_dwordx4 v[36:39], v229, s[20:23], s35 offen sc1 nt\n"
  "v_exp_f32 v137, v137\n"
  "v_fma_f32 v139, v139, s38, v209\n"
  "s_add_u32 s35, s35, 0x4000\n"
  "buffer_store_dwordx4 v[40:43], v229, s[20:23], s35 offen sc1 nt\n"
  "v_exp_f32 v138, v138\n"
  "v_cvt_pk_bf16_f32 v136, v136, v137\n"
  "s_add_u32 s35, s35, 0x4000\n"
  "buffer_store_dwordx4 v[44:47], v229, s[20:23], s35 offen sc1 nt\n"
  "v_fma_f32 v140, v140, s38, v209\n"
  "v_exp_f32 v139, v139\n"
  "v_mfma_f32_32x32x8_bf16 v[64:79], v[236:237], v[236:237], 0\n"
  "v_mfma_f32_32x32x8_bf16 v[80:95], v[236:237], v[236:237], 0\n"
  "v_fma_f32 v141, v141, s38, v209\n"
  "v_exp_f32 v140, v140\n"
  "buffer_store_dwordx4 v[96:99], v229, s[20:23], s37 offen sc1 nt\n"
  "v_cvt_pk_bf16_f32 v137, v138, v139\n"
  "v_fma_f32 v142, v142, s38, v209\n"
  "s_add_u32 s37, s37, 0x4000\n"
  "buffer_store_dwordx4 v[100:103], v229, s[20:23], s37 offen sc1 nt\n"
  "v_exp_f32 v141, v141\n"
  "v_fma_f32 v143, v143, s38, v209\n"
  "s_add_u32 s37, s37, 0x4000\n"
  "buffer_store_dwordx4 v[104:107], v229, s[20:23], s37 offen sc1 nt\n"
  "v_exp_f32 v142, v142\n"
  "v_cvt_pk_bf16_f32 v138, v140, v141\n"
  "s_add_u32 s37, s37, 0x4000\n"
  "buffer_store_dwordx4 v[108:111], v229, s[20:23], s37 offen sc1 nt\n"
  "v_exp_f32 v143, v143\n"
  "s_nop 0\n"
  "v_cvt_pk_bf16_f32 v139, v142, v143\n"
  "v_mfma_f32_32x32x8_bf16 v[32:47], v[236:237], v[236:237], 0\n"
  "v_mfma_f32_32x32x8_bf16 v[48:63], v[236:237], v[236:237], 0\n"
  "v_mfma_f32_32x32x8_bf16 v[96:111], v[236:237], v[236:237], 0\n"
  "v_mfma_f32_32x32x8_bf16 v[112:127], v[236:237], v[236:237], 0\n"
  "s_branch L_H2\n"
  "L_Hp:\n"
  "v_mfma_f32_16x16x32_bf16 v[128:131], a[192:195], a[0:3], 0\n"
  "v_cvt_pk_bf16_f32 v0, v0, v1\n"
  "v_cvt_pk_bf16_f32 v1, v2, v3\n"
  "v_mfma_f32_16x16x32_bf16 v[128:131], a[196:199], a[4:7], v[128:131]\n"
  "v_cvt_pk_bf16_f32 v2, v4, v5\n"
  "v_cvt_pk_bf16_f32 v3, v6, v7\n"
  "v_mfma_f32_16x16x32_bf16 v[128:131], a[200:203], a[8:11], v[128:131]\n"
  "v_cvt_pk_bf16_f32 v8, v8, v9\n"
  "v_cvt_pk_bf16_f32 v9, v10, v11\n"
  "v_mfma_f32_16x16x32_bf16 v[128:131], a[204:207], a[12:15], v[128:131]\n"
  "v_cvt_pk_bf16_f32 v10, v12, v13\n"
  "v_cvt_pk_bf16_f32 v11, v14, v15\n"
  "s_add_u32 s88, s77, s81\n"
  "v_mfma_f32_16x16x32_bf16 v[132:135], a[208:211], a[0:3], 0\n"
  "buffer_store_dwordx4 v[0:3], v226, s[24:27], s88 offen offset:0 sc1\n"
  "v_cvt_pk_bf16_f32 v16, v16, v17\n"
  "v_mfma_f32_16x16x32_bf16 v[132:135], a[212:215], a[4:7], v[132:135]\n"
  "v_cvt_pk_bf16_f32 v17, v18, v19\n"
  "v_cvt_pk_bf16_f32 v18, v20, v21\n"
  "v_mfma_f32_16x16x32_bf16 v[132:135], a[216:219], a[8:11], v[132:135]\n"
  "v_cvt_pk_bf16_f32 v19, v22, v23\n"
  "v_cvt_pk_bf16_f32 v24, v24, v25\n"
  "v_mfma_f32_16x16x32_bf16 v[132:135], a[220:223], a[12:15], v[132:135]\n"
  "v_cvt_pk_bf16_f32 v25, v26, v27\n"
  "v_cvt_pk_bf16_f32 v26, v28, v29\n"
  "v_mfma_f32_16x16x32_bf16 v[136:139], a[192:195], a[16:19], 0\n"
  "buffer_store_dwordx4 v[8:11], v226, s[24:27], s88 offen offset:1024 sc1\n"
  "v_cvt_pk_bf16_f32 v27, v30, v31\n"
  "v_mfma_f32_16x16x32_bf16 v[136:139], a[196:199], a[20:23], v[136:139]\n"
  "v_cvt_pk_bf16_f32 v32, v32, v33\n"
  "v_cvt_pk_bf16_f32 v33, v34, v35\n"
  "v_mfma_f32_16x16x32_bf16 v[136:139], a[200:203], a[24:27], v[136:139]\n"
  "v_cvt_pk_bf16_f32 v34, v36, v37\n"
  "v_cvt_pk_bf16_f32 v35, v38, v39\n"
  "v_mfma_f32_16x16x32_bf16 v[136:139], a[204:207], a[28:31], v[136:139]\n"
  "v_cvt_pk_bf16_f32 v40, v40, v41\n"
  "v_cvt_pk_bf16_f32 v41, v42, v43\n"
  "v_mfma_f32_16x16x32_bf16 v[140:143], a[208:211], a[16:19], 0\n"
  "buffer_store_dwordx4 v[16:19], v226, s[24:27], s88 offen offset:2048 sc1\n"
  "v_cvt_pk_bf16_f32 v42, v44, v45\n"
  "v_mfma_f32_16x16x32_bf16 v[140:143], a[212:215], a[20:23], v[140:143]\n"
  "v_cvt_pk_bf16_f32 v43, v46, v47\n"
  "v_fma_f32 v184, v184, s38, v211\n"
  "v_mfma_f32_16x16x32_bf16 v[140:143], a[216:219], a[24:27], v[140:143]\n"
  "v_fma_f32 v185, v185, s38, v211\n"
  "v_mfma_f32_16x16x32_bf16 v[140:143], a[220:223], a[28:31], v[140:143]\n"
  "v_exp_f32 v184, v184\n"
  "v_mfma_f32_16x16x32_bf16 v[144:147], a[192:195], a[32:35], 0\n"
  "buffer_store_dwordx4 v[24:27], v226, s[24:27], s88 offen offset:3072 sc1\n"
  "v_fma_f32 v186, v186, s38, v211\n"
  "v_mfma_f32_16x16x32_bf16 v[144:147], a[196:199], a[36:39], v[144:147]\n"
  "v_exp_f32 v185, v185\n"
  "v_mfma_f32_16x16x32_bf16 v[144:147], a[200:203], a[40:43], v[144:147]\n"
  "v_fma_f32 v187, v187, s38, v211\n"
  "v_mfma_f32_16x16x32_bf16 v[144:147], a[204:207], a[44:47], v[144:147]\n"
  "v_exp_f32 v186, v186\n"
  "s_add_u32 s88, s88, 4096\n"
  "v_mfma_f32_16x16x32_bf16 v[148:151], a[208:211], a[32:35], 0\n"
  "buffer_store_dwordx4 v[32:35], v226, s[24:27], s88 offen offset:0 sc1\n"
  "v_cvt_pk_bf16_f32 v184, v184, v185\n"
  "v_mfma_f32_16x16x32_bf16 v[148:151], a[212:215], a[36:39], v[148:151]\n"
  "v_fma_f32 v188, v188, s38, v211\n"
  "v_mfma_f32_16x16x32_bf16 v[148:151], a[216:219], a[40:43], v[148:151]\n"
  "v_exp_f32 v187, v187\n"
  "v_mfma_f32_16x16x32_bf16 v[148:151], a[220:223], a[44:47], v[148:151]\n"
  "v_cvt_pk_bf16_f32 v48, v48, v49\n"
  "v_cvt_pk_bf16_f32 v49, v50, v51\n"
  "v_mfma_f32_16x16x32_bf16 v[152:155], a[192:195], a[48:51], 0\n"
  "buffer_store_dwordx4 v[40:43], v226, s[24:27], s88 offen offset:1024 sc1\n"
  "v_cvt_pk_bf16_f32 v50, v52, v53\n"
  "v_mfma_f32_16x16x32_bf16 v[152:155], a[196:199], a[52:55], v[152:155]\n"
  "v_cvt_pk_bf16_f32 v51, v54, v55\n"
  "v_cvt_pk_bf16_f32 v56, v56, v57\n"
  "v_mfma_f32_16x16x32_bf16 v[152:155], a[200:203], a[56:59], v[152:155]\n"
  "v_cvt_pk_bf16_f32 v57, v58, v59\n"
  "v_cvt_pk_bf16_f32 v58, v60, v61\n"
  "v_mfma_f32_16x16x32_bf16 v[152:155], a[204:207], a[60:63], v[152:155]\n"
  "v_cvt_pk_bf16_f32 v59, v62, v63\n"
  "v_fma_f32 v189, v189, s38, v211\n"
  "v_mfma_f32_16x16x32_bf16 v[156:159], a[208:211], a[48:51], 0\n"
  "buffer_store_dwordx4 v[48:51], v226, s[24:27], s88 offen offset:2048 sc1\n"
  "v_mfma_f32_16x16x32_bf16 v[156:159], a[212:215], a[52:55], v[156:159]\n"
  "v_exp_f32 v188, v188\n"
  "v_mfma_f32_16x16x32_bf16 v[156:159], a[216:219], a[56:59], v[156:159]\n"
  "v_cvt_pk_bf16_f32 v185, v186, v187\n"
  "v_fma_f32 v190, v190, s38, v211\n"
  "v_mfma_f32_16x16x32_bf16 v[156:159], a[220:223], a[60:63], v[156:159]\n"
  "v_exp_f32 v189, v189\n"
  "v_fma_f32 v191, v191, s38, v211\n"
  "v_exp_f32 v190, v190\n"
  "v_cvt_pk_bf16_f32 v186, v188, v189\n"
  "v_exp_f32 v191, v191\n"
  "s_nop 0\n"
  "v_cvt_pk_bf16_f32 v187, v190, v191\n"
  "v_mov_b32 v212, 0x3f803f80\n"
  "v_mov_b32 v213, 0x3f803f80\n"
  "v_mov_b32 v214, 0x3f803f80\n"
  "v_mov_b32 v215, 0x3f803f80\n"
  "v_mfma_f32_16x16x32_bf16 v[200:203], v[216:219], v[176:179], v[200:203]\n"
  "ds_read_b128 a[192:195], v220 offset:0\n"
  "v_max3_f32 v240, v128, v129, v130\n"
  "v_mfma_f32_16x16x32_bf16 v[64:67], a[224:227], v[176:179], v[64:67]\n"
  "v_max3_f32 v242, v136, v137, v138\n"
  "v_max3_f32 v244, v144, v145, v146\n"
  "v_mfma_f32_16x16x32_bf16 v[68:71], a[228:231], v[176:179], v[68:71]\n"
  "ds_read_b128 a[196:199], v220 offset:512\n"
  "v_max3_f32 v246, v152, v153, v154\n"
  "v_mfma_f32_16x16x32_bf16 v[72:75], a[232:235], v[176:179], v[72:75]\n"
  "v_max_f32 v240, v240, v131\n"
  "v_max_f32 v242, v242, v139\n"
  "v_mfma_f32_16x16x32_bf16 v[76:79], a[236:239], v[176:179], v[76:79]\n"
  "ds_read_b128 a[200:203], v220 offset:1024\n"
  "v_max_f32 v244, v244, v147\n"
  "v_mfma_f32_16x16x32_bf16 v[80:83], a[240:243], v[176:179], v[80:83]\n"
  "v_max_f32 v246, v246, v155\n"
  "v_mov_b32 v241, v240\n"
  "v_mfma_f32_16x16x32_bf16 v[84:87], a[244:247], v[176:179], v[84:87]\n"
  "ds_read_b128 a[204:207], v220 offset:1536\n"
  "v_mov_b32 v243, v242\n"
  "v_mfma_f32_16x16x32_bf16 v[88:91], a[248:251], v[176:179], v[88:91]\n"
  "v_mov_b32 v245, v244\n"
  "v_mov_b32 v247, v246\n"
  "v_mfma_f32_16x16x32_bf16 v[92:95], a[252:255], v[176:179], v[92:95]\n"
  "buffer_store_dwordx4 v[56:59], v226, s[24:27], s88 offen offset:3072 sc1\n"
  "v_mfma_f32_16x16x32_bf16 v[204:207], v[216:219], v[184:187], v[204:207]\n"
  "v_permlane32_swap_b32 v241, v240\n"
  "v_mfma_f32_16x16x32_bf16 v[96:99], a[224:227], v[184:187], v[96:99]\n"
  "ds_read_b128 a[208:211], v220 offset:4096\n"
  "v_mfma_f32_16x16x32_bf16 v[100:103], a[228:231], v[184:187], v[100:103]\n"
  "v_permlane32_swap_b32 v243, v242\n"
  "v_mfma_f32_16x16x32_bf16 v[104:107], a[232:235], v[184:187], v[104:107]\n"
  "ds_read_b128 a[212:215], v220 offset:4608\n"
  "v_mfma_f32_16x16x32_bf16 v[108:111], a[236:239], v[184:187], v[108:111]\n"
  "v_permlane32_swap_b32 v245, v244\n"
  "v_mfma_f32_16x16x32_bf16 v[112:115], a[240:243], v[184:187], v[112:115]\n"
  "ds_read_b128 a[216:219], v220 offset:5120\n"
  "v_mfma_f32_16x16x32_bf16 v[116:119], a[244:247], v[184:187], v[116:119]\n"
  "v_permlane32_swap_b32 v247, v246\n"
  "v_mfma_f32_16x16x32_bf16 v[120:123], a[248:251], v[184:187], v[120:123]\n"
  "ds_read_b128 a[220:223], v220 offset:5632\n"
  "v_max_f32 v240, v240, v241\n"
  "v_mfma_f32_16x16x32_bf16 v[124:127], a[252:255], v[184:187], v[124:127]\n"
  "v_max_f32 v242, v242, v243\n"
  "v_max_f32 v244, v244, v245\n"
  "v_max_f32 v246, v246, v247\n"
  "v_mov_b32 v241, v240\n"
  "v_mov_b32 v243, v242\n"
  "v_mov_b32 v245, v244\n"
  "v_mov_b32 v247, v246\n"
  "v_permlane16_swap_b32 v241, v240\n"
  "v_permlane16_swap_b32 v243, v242\n"
  "v_permlane16_swap_b32 v245, v244\n"
  "v_permlane16_swap_b32 v247, v246\n"
  "v_max_f32 v240, v240, v241\n"
  "v_max_f32 v242, v242, v243\n"
  "v_max_f32 v244, v244, v245\n"
  "v_max_f32 v246, v246, v247\n"
  "v_mul_f32 v208, -v240, s38\n"
  "v_mul_f32 v209, -v242, s38\n"
  "v_mul_f32 v210, -v244, s38\n"
  "v_mul_f32 v211, -v246, s38\n"
  "v_mov_b32 v216, 0x3f803f80\n"
  "v_mov_b32 v217, 0x3f803f80\n"
  "v_mov_b32 v218, 0x3f803f80\n"
  "v_mov_b32 v219, 0x3f803f80\n"
  "v_cvt_pk_bf16_f32 v64, v64, v65\n"
  "v_cvt_pk_bf16_f32 v65, v66, v67\n"
  "v_cvt_pk_bf16_f32 v66, v68, v69\n"
  "v_cvt_pk_bf16_f32 v67, v70, v71\n"
  "v_cvt_pk_bf16_f32 v72, v72, v73\n"
  "v_cvt_pk_bf16_f32 v73, v74, v75\n"
  "v_cvt_pk_bf16_f32 v74, v76, v77\n"
  "v_cvt_pk_bf16_f32 v75, v78, v79\n"
  "v_cvt_pk_bf16_f32 v80, v80, v81\n"
  "v_cvt_pk_bf16_f32 v81, v82, v83\n"
  "v_cvt_pk_bf16_f32 v82, v84, v85\n"
  "v_cvt_pk_bf16_f32 v83, v86, v87\n"
  "v_cvt_pk_bf16_f32 v88, v88, v89\n"
  "v_cvt_pk_bf16_f32 v89, v90, v91\n"
  "v_cvt_pk_bf16_f32 v90, v92, v93\n"
  "v_cvt_pk_bf16_f32 v91, v94, v95\n"
  "v_mfma_f32_32x32x8_bf16 v[0:15], v[236:237], v[236:237], 0\n"
  "v_fma_f32 v128, v128, s38, v208\n"
  "v_fma_f32 v129, v129, s38, v208\n"
  "v_exp_f32 v128, v128\n"
  "v_fma_f32 v130, v130, s38, v208\n"
  "v_exp_f32 v129, v129\n"
  "v_fma_f32 v131, v131, s38, v208\n"
  "v_exp_f32 v130, v130\n"
  "s_add_u32 s88, s88, 4096\n"
  "buffer_store_dwordx4 v[64:67], v226, s[24:27], s88 offen offset:0 sc1\n"
  "v_cvt_pk_bf16_f32 v128, v128, v129\n"
  "v_fma_f32 v132, v132, s38, v208\n"
  "v_exp_f32 v131, v131\n"
  "v_mfma_f32_32x32x8_bf16 v[16:31], v[236:237], v[236:237], 0\n"
  "v_cvt_pk_bf16_f32 v96, v96, v97\n"
  "v_cvt_pk_bf16_f32 v97, v98, v99\n"
  "v_cvt_pk_bf16_f32 v98, v100, v101\n"
  "buffer_store_dwordx4 v[72:75], v226, s[24:27], s88 offen offset:1024 sc1\n"
  "v_cvt_pk_bf16_f32 v99, v102, v103\n"
  "v_cvt_pk_bf16_f32 v104, v104, v105\n"
  "v_cvt_pk_bf16_f32 v105, v106, v107\n"
  "v_cvt_pk_bf16_f32 v106, v108, v109\n"
  "v_cvt_pk_bf16_f32 v107, v110, v111\n"
  "v_mfma_f32_32x32x8_bf16 v[32:47], v[236:237], v[236:237], 0\n"
  "v_cvt_pk_bf16_f32 v112, v112, v113\n"
  "buffer_store_dwordx4 v[80:83], v226, s[24:27], s88 offen offset:2048 sc1\n"
  "v_cvt_pk_bf16_f32 v113, v114, v115\n"
  "v_cvt_pk_bf16_f32 v114, v116, v117\n"
  "v_cvt_pk_bf16_f32 v115, v118, v119\n"
  "v_cvt_pk_bf16_f32 v120, v120, v121\n"
  "v_cvt_pk_bf16_f32 v121, v122, v123\n"
  "v_cvt_pk_bf16_f32 v122, v124, v125\n"
  "v_cvt_pk_bf16_f32 v123, v126, v127\n"
  "buffer_store_dwordx4 v[88:91], v226, s[24:27], s88 offen offset:3072 sc1\n"
  "v_mfma_f32_32x32x8_bf16 v[48:63], v[236:237], v[236:237], 0\n"
  "v_mov_b32 v193, v196\n"
  "v_mov_b32 v194, v200\n"
  "v_mov_b32 v195, v204\n"
  "v_fma_f32 v133, v133, s38, v208\n"
  "v_exp_f32 v132, v132\n"
  "v_cvt_pk_bf16_f32 v129, v130, v131\n"
  "v_fma_f32 v134, v134, s38, v208\n"
  "v_exp_f32 v133, v133\n"
  "v_fma_f32 v135, v135, s38, v208\n"
  "s_add_u32 s88, s88, 4096\n"
  "buffer_store_dwordx4 v[96:99], v226, s[24:27], s88 offen offset:0 sc1\n"
  "v_exp_f32 v134, v134\n"
  "v_cvt_pk_bf16_f32 v130, v132, v133\n"
  "v_fma_f32 v136, v136, s38, v209\n"
  "v_exp_f32 v135, v135\n"
  "v_mfma_f32_32x32x8_bf16 v[64:79], v[236:237], v[236:237], 0\n"
  "v_fma_f32 v137, v137, s38, v209\n"
  "buffer_store_dwordx4 v[104:107], v226, s[24:27], s88 offen offset:1024 sc1\n"
  "v_exp_f32 v136, v136\n"
  "v_cvt_pk_bf16_f32 v131, v134, v135\n"
  "v_fma_f32 v138, v138, s38, v209\n"
  "v_exp_f32 v137, v137\n"
  "v_fma_f32 v139, v139, s38, v209\n"
  "v_exp_f32 v138, v138\n"
  "buffer_store_dwordx4 v[112:115], v226, s[24:27], s88 offen offset:2048 sc1\n"
  "v_cvt_pk_bf16_f32 v136, v136, v137\n"
  "v_fma_f32 v140, v140, s38, v209\n"
  "v_exp_f32 v139, v139\n"
  "v_mfma_f32_32x32x8_bf16 v[80:95], v[236:237], v[236:237], 0\n"
  "v_fma_f32 v141, v141, s38, v209\n"
  "v_exp_f32 v140, v140\n"
  "buffer_store_dwordx4 v[120:123], v226, s[24:27], s88 offen offset:3072 sc1\n"
  "v_cvt_pk_bf16_f32 v137, v138, v139\n"
  "v_fma_f32 v142, v142, s38, v209\n"
  "v_exp_f32 v141, v141\n"
  "v_fma_f32 v143, v143, s38, v209\n"
  "v_exp_f32 v142, v142\n"
  "v_cvt_pk_bf16_f32 v138, v140, v141\n"
  "s_lshl_b32 s88, s53, 10\n"
  "s_add_u32 s88, s77, s88\n"
  "s_add_u32 s88, s88, 65536\n"
  "buffer_store_dwordx4 v[192:195], v226, s[24:27], s88 offen sc1\n"
  "v_exp_f32 v143, v143\n"
  "s_nop 0\n"
  "v_cvt_pk_bf16_f32 v139, v142, v143\n"
  "v_mfma_f32_32x32x8_bf16 v[96:111], v[236:237], v[236:237], 0\n"
  "v_mfma_f32_32x32x8_bf16 v[112:127], v[236:237], v[236:237], 0\n"
  "s_add_u32 s88, s77, 69632\n"
  "s_cmp_eq_u32 s53, 0\n"
  "s_cselect_b32 s51, s88, 2146435072\n"
  "L_H2:\n"
  "v_mfma_f32_32x32x8_bf16 v[192:207], v[236:237], v[236:237], 0\n"
  "s_add_u32 s58, s70, 6\n"
  "s_add_u32 s59, s70, 7\n"
  "s_add_u32 s60, s70, 3\n"
  "s_add_u32 s61, s70, 5\n"
  "v_readlane_b32 s77, v228, s58\n"
  "v_readlane_b32 s73, v228, s59\n"
  "v_readlane_b32 s48, v228, s60\n"
  "v_readlane_b32 s72, v228, s61\n"
  "s_mov_b32 s22, s18\n"
  "s_mov_b64 s[34:35], s[28:29]\n"
  "s_mov_b64 s[36:37], s[30:31]\n"
  "s_mov_b32 s78, s79\n"
  "s_and_b32 s74, s73, 0xff\n"
  "s_andn2_b32 s73, s73, 0xff\n"
  "s_mov_b32 s75, 0\n"
  "s_max_u32 s87, s74, 1\n"
  "s_sub_u32 s87, s87, 1\n"
  "s_cmp_eq_u32 s78, 4\n"
  "s_cselect_b32 s76, 0, 2\n"
  "s_cmp_eq_u32 s74, 0\n"
  "s_cselect_b32 s76, 2, s76\n"
  "s_cmp_gt_u32 s74, 2\n"
  "s_cselect_b32 s76, 2, s76\n"
  "s_add_u32 s58, s73, 69632\n"
  "s_cmp_lg_u32 s74, 0\n"
  "s_cselect_b32 s50, s58, 2146435072\n"
  "s_lshr_b32 s80, s70, 3\n"
  "s_and_b32 s48, s48, 0xffff\n"
  "s_sub_u32 s48, s48, 1\n"
  "s_mov_b32 s83, s48\n"
  "v_mov_b32 v230, 0\n"
  "v_mov_b32 v231, 0\n"
  "s_cmp_lt_u32 s72, 32\n"
  "s_cselect_b32 s60, 1, 0\n"
  "s_sub_u32 s49, s48, s60\n"
  "s_cbranch_scc0 L_H_nm\n"
  "v_lshrrev_b32 v235, 8, v226\n"
  "s_mul_i32 s58, s72, 15\n"
  "s_mov_b32 s59, 0x3f803f80\n"
  "v_mul_u32_u24 v235, 60, v235\n"
  "v_sub_u32 v235, s58, v235\n"
  "v_subrev_u32 v212, 0, v235\n"
  "v_subrev_u32 v213, 30, v235\n"
  "v_subrev_u32 v214, 240, v235\n"
  "v_subrev_u32 v215, 270, v235\n"
  "v_med3_i32 v212, v212, 0, 30\n"
  "v_med3_i32 v213, v213, 0, 30\n"
  "v_med3_i32 v214, v214, 0, 30\n"
  "v_med3_i32 v215, v215, 0, 30\n"
  "v_bfe_u32 v212, s59, 0, v212\n"
  "v_bfe_u32 v213, s59, 0, v213\n"
  "v_bfe_u32 v214, s59, 0, v214\n"
  "v_bfe_u32 v215, s59, 0, v215\n"
  "s_mov_b32 s49, 0\n"
  "L_H_nm:\n"
  "s_min_u32 s58, s49, s47\n"
  "s_sub_i32 s59, s48, 3\n"
  "s_max_i32 s59, s59, 0\n"
  "s_cmp_eq_u32 s76, 0\n"
  "s_cselect_b32 s59, s59, s58\n"
  "s_min_u32 s58, s58, s59\n"
  "s_sub_u32 s48, s48, s58\n"
  "s_sub_u32 s49, s49, s58\n"
  "s_sub_u32 s47, s47, s58\n"
  "s_sub_u32 s44, s58, 0\n"
  "s_add_u32 s62, s70, 8\n"
  "s_cmp_ge_u32 s62, s55\n"
  "s_cbranch_scc1 L_nopfH\n"
  "s_add_u32 s58, s62, 0\n"
  "s_add_u32 s59, s62, 1\n"
  "s_add_u32 s61, s62, 3\n"
  "v_readlane_b32 s79, v228, s61\n"
  "v_readlane_b32 s28, v228, s58\n"
  "v_readlane_b32 s18, v228, s59\n"
  "s_lshr_b32 s79, s79, 16\n"
  "s_mul_i32 s61, s53, s79\n"
  "s_lshl_b32 s61, s61, 16\n"
  "s_add_u32 s28, s28, s61\n"
  "s_add_u32 s58, s28, 0x10000\n"
  "s_add_u32 s59, s28, 0x20000\n"
  "s_add_u32 s60, s28, 0x30000\n"
  "s_cmp_ge_u32 s79, 3\n"
  "s_cselect_b32 s29, s58, 2146435072\n"
  "s_cselect_b32 s30, s59, s58\n"
  "s_cmp_ge_u32 s79, 2\n"
  "s_cselect_b32 s30, s30, 2146435072\n"
  "s_cmp_ge_u32 s79, 4\n"
  "s_cselect_b32 s31, s60, 2146435072\n"
  "s_cmp_lg_u32 s44, 0\n"
  "s_cselect_b32 s58, s78, 0\n"
  "s_cmp_eq_u32 s58, 4\n"
  "s_cselect_b32 s78, 5, s78\n"
  "s_cbranch_scc1 L_nopfH\n"
  "s_add_u32 m0, s82, 0\n"
  "s_nop 0\n"
  "buffer_load_dwordx4 v222, s[16:19], s28 offen offset:0 lds\n"
  "buffer_load_dwordx4 v223, s[16:19], s28 offen offset:1024 lds\n"
  "buffer_load_dwordx4 v224, s[16:19], s28 offen offset:2048 lds\n"
  "buffer_load_dwordx4 v225, s[16:19], s28 offen offset:3072 lds\n"
  "s_add_u32 m0, s82, 4096\n"
  "s_nop 0\n"
  "buffer_load_dwordx4 v222, s[16:19], s29 offen offset:0 lds\n"
  "buffer_load_dwordx4 v223, s[16:19], s29 offen offset:1024 lds\n"
  "buffer_load_dwordx4 v224, s[16:19], s29 offen offset:2048 lds\n"
  "buffer_load_dwordx4 v225, s[16:19], s29 offen offset:3072 lds\n"
  "s_add_u32 m0, s82, 8192\n"
  "s_nop 0\n"
  "buffer_load_dwordx4 v222, s[16:19], s30 offen offset:0 lds\n"
  "buffer_load_dwordx4 v223, s[16:19], s30 offen offset:1024 lds\n"
  "buffer_load_dwordx4 v224, s[16:19], s30 offen offset:2048 lds\n"
  "buffer_load_dwordx4 v225, s[16:19], s30 offen offset:3072 lds\n"
  "s_add_u32 m0, s82, 12288\n"
  "s_nop 0\n"
  "buffer_load_dwordx4 v222, s[16:19], s31 offen offset:0 lds\n"
  "buffer_load_dwordx4 v223, s[16:19], s31 offen offset:1024 lds\n"
  "buffer_load_dwordx4 v224, s[16:19], s31 offen offset:2048 lds\n"
  "buffer_load_dwordx4 v225, s[16:19], s31 offen offset:3072 lds\n"
  "L_nopfH:\n"
  "s_cmp_ge_u32 s78, 3\n"
  "s_cbranch_scc1 L_hrH\n"
  "s_or_b32 m0, s45, s43\n"
  "s_nop 0\n"
  "buffer_load_dwordx4 v222, s[8:11], s39 offen offset:0 lds\n"
  "buffer_load_dwordx4 v223, s[8:11], s39 offen offset:1024 lds\n"
  "buffer_load_dwordx4 v224, s[8:11], s39 offen offset:2048 lds\n"
  "buffer_load_dwordx4 v225, s[8:11], s39 offen offset:3072 lds\n"
  "s_or_b32 m0, s46, s43\n"
  "s_nop 0\n"
  "buffer_load_dwordx4 v222, s[12:15], s41 offen offset:0 lds\n"
  "buffer_load_dwordx4 v223, s[12:15], s41 offen offset:1024 lds\n"
  "buffer_load_dwordx4 v224, s[12:15], s41 offen offset:2048 lds\n"
  "buffer_load_dwordx4 v225, s[12:15], s41 offen offset:3072 lds\n"
  "L_hrH:\n"
  "s_waitcnt lgkmcnt(0)\n"
  "s_cmp_eq_u32 s78, 5\n"
  "s_cselect_b32 s78, 4, s78\n"
  "s_cbranch_scc1 L_loop4q_G2\n"
  "s_cmp_eq_u32 s78, 4\n"
  "s_cbranch_scc1 L_loop4_G2\n"
  "s_cmp_eq_u32 s78, 3\n"
  "s_cbranch_scc1 L_loop3_G2\n"
  "s_cmp_eq_u32 s78, 2\n"
  "s_cbranch_scc1 L_loop2_G2\n"
  "s_branch L_loop1_G2\n"
  "L_loop4p:\n"
  "v_mfma_f32_16x16x32_bf16 v[128:131], a[192:195], a[0:3], 0\n"
  "s_barrier\n"
  "ds_read_b64_tr_b16 a[224:225], v221 offset:0\n"
  "v_fma_f32 v176, v176, s38, v210\n"
  "v_mfma_f32_16x16x32_bf16 v[128:131], a[196:199], a[4:7], v[128:131]\n"
  "ds_read_b64_tr_b16 a[226:227], v221 offset:4096\n"
  "v_exp_f32 v176, v176\n"
  "v_mfma_f32_16x16x32_bf16 v[128:131], a[200:203], a[8:11], v[128:131]\n"
  "ds_read_b64_tr_b16 a[228:229], v239 offset:0\n"
  "v_fma_f32 v177, v177, s38, v210\n"
  "v_fma_f32 v178, v178, s38, v210\n"
  "v_mfma_f32_16x16x32_bf16 v[128:131], a[204:207], a[12:15], v[128:131]\n"
  "ds_read_b64_tr_b16 a[230:231], v239 offset:4096\n"
  "v_exp_f32 v177, v177\n"
  "v_mfma_f32_16x16x32_bf16 v[132:135], a[208:211], a[0:3], 0\n"
  "ds_read_b64_tr_b16 a[232:233], v221 offset:512\n"
  "v_cvt_pk_bf16_f32 v176, v176, v177\n"
  "v_fma_f32 v179, v179, s38, v210\n"
  "v_mfma_f32_16x16x32_bf16 v[132:135], a[212:215], a[4:7], v[132:135]\n"
  "buffer_store_dwordx2 v[232:233], v227, s[24:27], s51 offen sc1\n"
  "v_fma_f32 v180, v180, s38, v210\n"
  "v_mfma_f32_16x16x32_bf16 v[132:135], a[216:219], a[8:11], v[132:135]\n"
  "ds_read_b64_tr_b16 a[234:235], v221 offset:4608\n"
  "v_exp_f32 v178, v178\n"
  "v_mfma_f32_16x16x32_bf16 v[132:135], a[220:223], a[12:15], v[132:135]\n"
  "ds_read_b64_tr_b16 a[236:237], v239 offset:512\n"
  "v_exp_f32 v179, v179\n"
  "v_mfma_f32_16x16x32_bf16 v[136:139], a[192:195], a[16:19], 0\n"
  "buffer_load_dwordx2 v[230:231], v227, s[24:27], s50 offen sc1\n"
  "v_cvt_pk_bf16_f32 v177, v178, v179\n"
  "v_mfma_f32_16x16x32_bf16 v[136:139], a[196:199], a[20:23], v[136:139]\n"
  "ds_read_b64_tr_b16 a[238:239], v239 offset:4608\n"
  "v_exp_f32 v180, v180\n"
  "v_mfma_f32_16x16x32_bf16 v[136:139], a[200:203], a[24:27], v[136:139]\n"
  "ds_read_b64_tr_b16 a[240:241], v221 offset:1024\n"
  "v_fma_f32 v181, v181, s38, v210\n"
  "v_fma_f32 v182, v182, s38, v210\n"
  "v_mfma_f32_16x16x32_bf16 v[136:139], a[204:207], a[28:31], v[136:139]\n"
  "s_lshl_b32 s88, s53, 10\n"
  "s_add_u32 s88, s73, s88\n"
  "s_add_u32 s88, s88, 65536\n"
  "v_mfma_f32_16x16x32_bf16 v[140:143], a[208:211], a[16:19], 0\n"
  "buffer_load_dwordx4 v[240:243], v226, s[24:27], s88 offen sc1\n"
  "v_fma_f32 v183, v183, s38, v210\n"
  "v_mfma_f32_16x16x32_bf16 v[140:143], a[212:215], a[20:23], v[140:143]\n"
  "ds_read_b64_tr_b16 a[242:243], v221 offset:5120\n"
  "v_exp_f32 v181, v181\n"
  "v_mfma_f32_16x16x32_bf16 v[140:143], a[216:219], a[24:27], v[140:143]\n"
  "s_add_u32 s73, s73, s81\n"
  "v_cvt_pk_bf16_f32 v178, v180, v181\n"
  "v_fma_f32 v184, v184, s38, v211\n"
  "v_mfma_f32_16x16x32_bf16 v[140:143], a[220:223], a[28:31], v[140:143]\n"
  "buffer_load_dwordx4 a[64:67], v226, s[24:27], s73 offen offset:0 sc1\n"
  "v_fma_f32 v185, v185, s38, v211\n"
  "v_mfma_f32_16x16x32_bf16 v[144:147], a[192:195], a[32:35], 0\n"
  "ds_read_b64_tr_b16 a[244:245], v239 offset:1024\n"
  "v_exp_f32 v182, v182\n"
  "v_mfma_f32_16x16x32_bf16 v[144:147], a[196:199], a[36:39], v[144:147]\n"
  "ds_read_b64_tr_b16 a[246:247], v239 offset:5120\n"
  "v_exp_f32 v183, v183\n"
  "v_mfma_f32_16x16x32_bf16 v[144:147], a[200:203], a[40:43], v[144:147]\n"
  "buffer_load_dwordx4 a[68:71], v226, s[24:27], s73 offen offset:1024 sc1\n"
  "v_cvt_pk_bf16_f32 v179, v182, v183\n"
  "v_mfma_f32_16x16x32_bf16 v[144:147], a[204:207], a[44:47], v[144:147]\n"
  "ds_read_b64_tr_b16 a[248:249], v221 offset:1536\n"
  "v_exp_f32 v184, v184\n"
  "v_mfma_f32_16x16x32_bf16 v[148:151], a[208:211], a[32:35], 0\n"
  "ds_read_b64_tr_b16 a[250:251], v221 offset:5632\n"
  "v_exp_f32 v185, v185\n"
  "v_mfma_f32_16x16x32_bf16 v[148:151], a[212:215], a[36:39], v[148:151]\n"
  "buffer_load_dwordx4 a[72:75], v226, s[24:27], s73 offen offset:2048 sc1\n"
  "ds_read_b64_tr_b16 a[252:253], v239 offset:1536\n"
  "v_mfma_f32_16x16x32_bf16 v[148:151], a[216:219], a[40:43], v[148:151]\n"
  "ds_read_b64_tr_b16 a[254:255], v239 offset:5632\n"
  "v_cvt_pk_bf16_f32 v184, v184, v185\n"
  "v_fma_f32 v186, v186, s38, v211\n"
  "v_mfma_f32_16x16x32_bf16 v[148:151], a[220:223], a[44:47], v[148:151]\n"
  "v_exp_f32 v186, v186\n"
  "v_fma_f32 v187, v187, s38, v211\n"
  "v_mfma_f32_16x16x32_bf16 v[152:155], a[192:195], a[48:51], 0\n"
  "v_exp_f32 v187, v187\n"
  "v_fma_f32 v188, v188, s38, v211\n"
  "v_mfma_f32_16x16x32_bf16 v[152:155], a[196:199], a[52:55], v[152:155]\n"
  "buffer_load_dwordx4 a[76:79], v226, s[24:27], s73 offen offset:3072 sc1\n"
  "v_cvt_pk_bf16_f32 v185, v186, v187\n"
  "v_mfma_f32_16x16x32_bf16 v[152:155], a[200:203], a[56:59], v[152:155]\n"
  "v_exp_f32 v188, v188\n"
  "v_fma_f32 v189, v189, s38, v211\n"
  "v_mfma_f32_16x16x32_bf16 v[152:155], a[204:207], a[60:63], v[152:155]\n"
  "s_add_u32 s73, s73, 4096\n"
  "v_exp_f32 v189, v189\n"
  "v_mfma_f32_16x16x32_bf16 v[156:159], a[208:211], a[48:51], 0\n"
  "buffer_load_dwordx4 a[80:83], v226, s[24:27], s73 offen offset:0 sc1\n"
  "v_cvt_pk_bf16_f32 v186, v188, v189\n"
  "v_mfma_f32_16x16x32_bf16 v[156:159], a[212:215], a[52:55], v[156:159]\n"
  "v_fma_f32 v190, v190, s38, v211\n"
  "v_fma_f32 v191, v191, s38, v211\n"
  "v_mfma_f32_16x16x32_bf16 v[156:159], a[216:219], a[56:59], v[156:159]\n"
  "v_exp_f32 v190, v190\n"
  "v_exp_f32 v191, v191\n"
  "v_mfma_f32_16x16x32_bf16 v[156:159], a[220:223], a[60:63], v[156:159]\n"
  "buffer_load_dwordx4 a[84:87], v226, s[24:27], s73 offen offset:1024 sc1\n"
  "v_cvt_pk_bf16_f32 v187, v190, v191\n"
  "s_waitcnt lgkmcnt(0)\n"
  "v_mfma_f32_16x16x32_bf16 v[192:195], v[216:219], v[160:163], v[192:195]\n"
  "ds_read_b128 a[192:195], v220 offset:0\n"
  "v_fma_f32 v128, v128, s38, v208\n"
  "v_mfma_f32_16x16x32_bf16 v[0:3], a[224:227], v[160:163], v[0:3]\n"
  "ds_read_b128 a[196:199], v220 offset:512\n"
  "v_exp_f32 v128, v128\n"
  "v_mfma_f32_16x16x32_bf16 v[4:7], a[228:231], v[160:163], v[4:7]\n"
  "ds_read_b128 a[200:203], v220 offset:1024\n"
  "v_fma_f32 v129, v129, s38, v208\n"
  "v_mfma_f32_16x16x32_bf16 v[8:11], a[232:235], v[160:163], v[8:11]\n"
  "buffer_load_dwordx4 a[88:91], v226, s[24:27], s73 offen offset:2048 sc1\n"
  "v_fma_f32 v130, v130, s38, v208\n"
  "v_mfma_f32_16x16x32_bf16 v[12:15], a[236:239], v[160:163], v[12:15]\n"
  "v_exp_f32 v129, v129\n"
  "v_mfma_f32_16x16x32_bf16 v[16:19], a[240:243], v[160:163], v[16:19]\n"
  "v_cvt_pk_bf16_f32 v128, v128, v129\n"
  "v_fma_f32 v131, v131, s38, v208\n"
  "v_mfma_f32_16x16x32_bf16 v[20:23], a[244:247], v[160:163], v[20:23]\n"
  "buffer_load_dwordx4 a[92:95], v226, s[24:27], s73 offen offset:3072 sc1\n"
  "v_fma_f32 v132, v132, s38, v208\n"
  "v_mfma_f32_16x16x32_bf16 v[24:27], a[248:251], v[160:163], v[24:27]\n"
  "v_exp_f32 v130, v130\n"
  "v_mfma_f32_16x16x32_bf16 v[28:31], a[252:255], v[160:163], v[28:31]\n"
  "s_add_u32 s73, s73, 4096\n"
  "v_fma_f32 v133, v133, s38, v208\n"
  "v_mfma_f32_16x16x32_bf16 v[196:199], v[216:219], v[168:171], v[196:199]\n"
  "buffer_load_dwordx4 a[96:99], v226, s[24:27], s73 offen offset:0 sc1\n"
  "v_fma_f32 v134, v134, s38, v208\n"
  "v_mfma_f32_16x16x32_bf16 v[32:35], a[224:227], v[168:171], v[32:35]\n"
  "v_exp_f32 v131, v131\n"
  "v_mfma_f32_16x16x32_bf16 v[36:39], a[228:231], v[168:171], v[36:39]\n"
  "ds_read_b128 a[204:207], v220 offset:1536\n"
  "v_cvt_pk_bf16_f32 v129, v130, v131\n"
  "v_fma_f32 v135, v135, s38, v208\n"
  "v_mfma_f32_16x16x32_bf16 v[40:43], a[232:235], v[168:171], v[40:43]\n"
  "buffer_load_dwordx4 a[100:103], v226, s[24:27], s73 offen offset:1024 sc1\n"
  "v_mfma_f32_16x16x32_bf16 v[44:47], a[236:239], v[168:171], v[44:47]\n"
  "v_exp_f32 v132, v132\n"
  "v_mfma_f32_16x16x32_bf16 v[48:51], a[240:243], v[168:171], v[48:51]\n"
  "ds_read_b128 a[208:211], v220 offset:4096\n"
  "v_exp_f32 v133, v133\n"
  "v_mfma_f32_16x16x32_bf16 v[52:55], a[244:247], v[168:171], v[52:55]\n"
  "v_cvt_pk_bf16_f32 v130, v132, v133\n"
  "v_fma_f32 v136, v136, s38, v209\n"
  "v_mfma_f32_16x16x32_bf16 v[56:59], a[248:251], v[168:171], v[56:59]\n"
  "buffer_load_dwordx4 a[104:107], v226, s[24:27], s73 offen offset:2048 sc1\n"
  "v_fma_f32 v137, v137, s38, v209\n"
  "v_mfma_f32_16x16x32_bf16 v[60:63], a[252:255], v[168:171], v[60:63]\n"
  "v_exp_f32 v134, v134\n"
  "v_mfma_f32_16x16x32_bf16 v[200:203], v[216:219], v[176:179], v[200:203]\n"
  "v_exp_f32 v135, v135\n"
  "v_mfma_f32_16x16x32_bf16 v[64:67], a[224:227], v[176:179], v[64:67]\n"
  "buffer_load_dwordx4 a[108:111], v226, s[24:27], s73 offen offset:3072 sc1\n"
  "v_cvt_pk_bf16_f32 v131, v134, v135\n"
  "v_mfma_f32_16x16x32_bf16 v[68:71], a[228:231], v[176:179], v[68:71]\n"
  "ds_read_b128 a[212:215], v220 offset:4608\n"
  "ds_read_b128 a[216:219], v220 offset:5120\n"
  "ds_read_b128 a[220:223], v220 offset:5632\n"
  "v_mfma_f32_16x16x32_bf16 v[72:75], a[232:235], v[176:179], v[72:75]\n"
  "s_add_u32 s73, s73, 4096\n"
  "v_fma_f32 v138, v138, s38, v209\n"
  "v_mfma_f32_16x16x32_bf16 v[76:79], a[236:239], v[176:179], v[76:79]\n"
  "buffer_load_dwordx4 a[112:115], v226, s[24:27], s73 offen offset:0 sc1\n"
  "v_mfma_f32_16x16x32_bf16 v[80:83], a[240:243], v[176:179], v[80:83]\n"
  "v_exp_f32 v136, v136\n"
  "v_mfma_f32_16x16x32_bf16 v[84:87], a[244:247], v[176:179], v[84:87]\n"
  "v_exp_f32 v137, v137\n"
  "v_fma_f32 v139, v139, s38, v209\n"
  "v_mfma_f32_16x16x32_bf16 v[88:91], a[248:251], v[176:179], v[88:91]\n"
  "v_cvt_pk_bf16_f32 v136, v136, v137\n"
  "v_fma_f32 v140, v140, s38, v209\n"
  "v_mfma_f32_16x16x32_bf16 v[92:95], a[252:255], v[176:179], v[92:95]\n"
  "buffer_load_dwordx4 a[116:119], v226, s[24:27], s73 offen offset:1024 sc1\n"
  "v_mfma_f32_16x16x32_bf16 v[204:207], v[216:219], v[184:187], v[204:207]\n"
  "v_exp_f32 v138, v138\n"
  "v_fma_f32 v141, v141, s38, v209\n"
  "v_mfma_f32_16x16x32_bf16 v[96:99], a[224:227], v[184:187], v[96:99]\n"
  "v_exp_f32 v139, v139\n"
  "v_mfma_f32_16x16x32_bf16 v[100:103], a[228:231], v[184:187], v[100:103]\n"
  "buffer_load_dwordx4 a[120:123], v226, s[24:27], s73 offen offset:2048 sc1\n"
  "v_cvt_pk_bf16_f32 v137, v138, v139\n"
  "v_mfma_f32_16x16x32_bf16 v[104:107], a[232:235], v[184:187], v[104:107]\n"
  "v_exp_f32 v140, v140\n"
  "v_mfma_f32_16x16x32_bf16 v[108:111], a[236:239], v[184:187], v[108:111]\n"
  "v_exp_f32 v141, v141\n"
  "v_mfma_f32_16x16x32_bf16 v[112:115], a[240:243], v[184:187], v[112:115]\n"
  "buffer_load_dwordx4 a[124:127], v226, s[24:27], s73 offen offset:3072 sc1\n"
  "v_cvt_pk_bf16_f32 v138, v140, v141\n"
  "v_mfma_f32_16x16x32_bf16 v[116:119], a[244:247], v[184:187], v[116:119]\n"
  "v_fma_f32 v142, v142, s38, v209\n"
  "v_fma_f32 v143, v143, s38, v209\n"
  "v_mfma_f32_16x16x32_bf16 v[120:123], a[248:251], v[184:187], v[120:123]\n"
  "v_exp_f32 v142, v142\n"
  "v_mfma_f32_16x16x32_bf16 v[124:127], a[252:255], v[184:187], v[124:127]\n"
  "v_exp_f32 v143, v143\n"
  "s_waitcnt lgkmcnt(0)\n"
  "v_cvt_pk_bf16_f32 v139, v142, v143\n"
  "s_branch L_loop4_G2\n"
  "L_Zf:\n"
  "s_cmp_ge_u32 s72, 64\n"
  "s_cbranch_scc1 L_m1Zf\n"
  "v_lshrrev_b32 v235, 8, v226\n"
  "s_mul_i32 s58, s72, 15\n"
  "s_mov_b32 s59, 0x3f803f80\n"
  "v_mul_u32_u24 v235, 60, v235\n"
  "v_sub_u32 v235, s58, v235\n"
  "v_subrev_u32 v216, 480, v235\n"
  "v_subrev_u32 v217, 510, v235\n"
  "v_subrev_u32 v218, 720, v235\n"
  "v_subrev_u32 v219, 750, v235\n"
  "v_med3_i32 v216, v216, 0, 30\n"
  "v_med3_i32 v217, v217, 0, 30\n"
  "v_med3_i32 v218, v218, 0, 30\n"
  "v_med3_i32 v219, v219, 0, 30\n"
  "v_bfe_u32 v216, s59, 0, v216\n"
  "v_bfe_u32 v217, s59, 0, v217\n"
  "v_bfe_u32 v218, s59, 0, v218\n"
  "v_bfe_u32 v219, s59, 0, v219\n"
  "L_m1Zf:\n"
  "v_and_b32 v128, 0xf0, v226\n"
  "v_and_b32 v134, 0x70, v226\n"
  "v_lshl_add_u32 v128, v128, 4, v134\n"
  "v_lshrrev_b32 v134, 5, v226\n"
  "v_and_b32 v134, 0x18, v134\n"
  "v_xor_b32 v128, v128, v134\n"
  "v_add_u32 v128, s82, v128\n"
  "v_xor_b32 v129, 32, v128\n"
  "v_xor_b32 v130, 64, v128\n"
  "v_xor_b32 v131, 96, v128\n"
  "v_lshrrev_b32 v134, 4, v226\n"
  "v_and_b32 v134, 0x30, v134\n"
  "v_xor_b32 v132, v226, v134\n"
  "v_add_u32 v132, s82, v132\n"
  "v_xor_b32 v133, 64, v132\n"
  "s_mov_b32 s88, 0x35800000\n"
  "v_mov_b32 v234, 0x67800000\n"
  "s_cmp_lg_u32 s74, 0\n"
  "s_cbranch_scc1 L_Zf1\n"
  "s_waitcnt lgkmcnt(0)\n"
  "v_mfma_f32_16x16x32_bf16 v[192:195], v[216:219], v[160:163], v[192:195]\n"
  "v_fma_f32 v176, v176, s38, v210\n"
  "v_fma_f32 v177, v177, s38, v210\n"
  "v_fma_f32 v178, v178, s38, v210\n"
  "v_mfma_f32_16x16x32_bf16 v[0:3], a[224:227], v[160:163], v[0:3]\n"
  "v_exp_f32 v176, v176\n"
  "v_fma_f32 v179, v179, s38, v210\n"
  "v_mfma_f32_16x16x32_bf16 v[4:7], a[228:231], v[160:163], v[4:7]\n"
  "v_exp_f32 v177, v177\n"
  "v_fma_f32 v180, v180, s38, v210\n"
  "v_mfma_f32_16x16x32_bf16 v[8:11], a[232:235], v[160:163], v[8:11]\n"
  "v_cvt_pk_bf16_f32 v176, v176, v177\n"
  "v_exp_f32 v178, v178\n"
  "v_mfma_f32_16x16x32_bf16 v[12:15], a[236:239], v[160:163], v[12:15]\n"
  "v_exp_f32 v179, v179\n"
  "v_exp_f32 v180, v180\n"
  "v_mfma_f32_16x16x32_bf16 v[16:19], a[240:243], v[160:163], v[16:19]\n"
  "v_cvt_pk_bf16_f32 v177, v178, v179\n"
  "v_fma_f32 v181, v181, s38, v210\n"
  "v_fma_f32 v182, v182, s38, v210\n"
  "v_mfma_f32_16x16x32_bf16 v[20:23], a[244:247], v[160:163], v[20:23]\n"
  "v_exp_f32 v181, v181\n"
  "v_fma_f32 v183, v183, s38, v210\n"
  "v_mfma_f32_16x16x32_bf16 v[24:27], a[248:251], v[160:163], v[24:27]\n"
  "v_cvt_pk_bf16_f32 v178, v180, v181\n"
  "v_exp_f32 v182, v182\n"
  "v_mfma_f32_16x16x32_bf16 v[28:31], a[252:255], v[160:163], v[28:31]\n"
  "v_exp_f32 v183, v183\n"
  "v_fma_f32 v184, v184, s38, v211\n"
  "v_mfma_f32_16x16x32_bf16 v[196:199], v[216:219], v[168:171], v[196:199]\n"
  "v_cvt_pk_bf16_f32 v179, v182, v183\n"
  "v_exp_f32 v184, v184\n"
  "v_mfma_f32_16x16x32_bf16 v[32:35], a[224:227], v[168:171], v[32:35]\n"
  "v_fma_f32 v185, v185, s38, v211\n"
  "v_fma_f32 v186, v186, s38, v211\n"
  "v_fma_f32 v187, v187, s38, v211\n"
  "v_mfma_f32_16x16x32_bf16 v[36:39], a[228:231], v[168:171], v[36:39]\n"
  "v_exp_f32 v185, v185\n"
  "v_fma_f32 v188, v188, s38, v211\n"
  "v_mfma_f32_16x16x32_bf16 v[40:43], a[232:235], v[168:171], v[40:43]\n"
  "v_cvt_pk_bf16_f32 v184, v184, v185\n"
  "v_exp_f32 v186, v186\n"
  "v_mfma_f32_16x16x32_bf16 v[44:47], a[236:239], v[168:171], v[44:47]\n"
  "v_exp_f32 v187, v187\n"
  "v_exp_f32 v188, v188\n"
  "v_mfma_f32_16x16x32_bf16 v[48:51], a[240:243], v[168:171], v[48:51]\n"
  "v_cvt_pk_bf16_f32 v185, v186, v187\n"
  "v_fma_f32 v189, v189, s38, v211\n"
  "v_fma_f32 v190, v190, s38, v211\n"
  "v_mfma_f32_16x16x32_bf16 v[52:55], a[244:247], v[168:171], v[52:55]\n"
  "v_exp_f32 v189, v189\n"
  "v_fma_f32 v191, v191, s38, v211\n"
  "v_mfma_f32_16x16x32_bf16 v[56:59], a[248:251], v[168:171], v[56:59]\n"
  "v_cvt_pk_bf16_f32 v186, v188, v189\n"
  "v_exp_f32 v190, v190\n"
  "v_mfma_f32_16x16x32_bf16 v[60:63], a[252:255], v[168:171], v[60:63]\n"
  "v_exp_f32 v191, v191\n"
  "v_med3_f32 v193, v192, s88, v234\n"
  "v_rcp_f32 v194, v192\n"
  "v_cmp_neq_f32 s[62:63], v193, v192\n"
  "v_med3_f32 v197, v196, s88, v234\n"
  "v_rcp_f32 v198, v196\n"
  "v_cmp_neq_f32 s[64:65], v197, v196\n"
  "v_cvt_pk_bf16_f32 v187, v190, v191\n"
  "v_pk_mul_f32 v[0:1], v[0:1], v[194:195] op_sel_hi:[1,0]\n"
  "v_pk_mul_f32 v[2:3], v[2:3], v[194:195] op_sel_hi:[1,0]\n"
  "v_pk_mul_f32 v[4:5], v[4:5], v[194:195] op_sel_hi:[1,0]\n"
  "v_pk_mul_f32 v[6:7], v[6:7], v[194:195] op_sel_hi:[1,0]\n"
  "v_pk_mul_f32 v[8:9], v[8:9], v[194:195] op_sel_hi:[1,0]\n"
  "v_pk_mul_f32 v[10:11], v[10:11], v[194:195] op_sel_hi:[1,0]\n"
  "v_pk_mul_f32 v[12:13], v[12:13], v[194:195] op_sel_hi:[1,0]\n"
  "v_pk_mul_f32 v[14:15], v[14:15], v[194:195] op_sel_hi:[1,0]\n"
  "v_pk_mul_f32 v[16:17], v[16:17], v[194:195] op_sel_hi:[1,0]\n"
  "v_pk_mul_f32 v[18:19], v[18:19], v[194:195] op_sel_hi:[1,0]\n"
  "v_pk_mul_f32 v[20:21], v[20:21], v[194:195] op_sel_hi:[1,0]\n"
  "v_pk_mul_f32 v[22:23], v[22:23], v[194:195] op_sel_hi:[1,0]\n"
  "v_pk_mul_f32 v[24:25], v[24:25], v[194:195] op_sel_hi:[1,0]\n"
  "v_pk_mul_f32 v[26:27], v[26:27], v[194:195] op_sel_hi:[1,0]\n"
  "v_pk_mul_f32 v[28:29], v[28:29], v[194:195] op_sel_hi:[1,0]\n"
  "v_pk_mul_f32 v[30:31], v[30:31], v[194:195] op_sel_hi:[1,0]\n"
  "v_cvt_pk_bf16_f32 v0, v0, v1\n"
  "v_cvt_pk_bf16_f32 v1, v2, v3\n"
  "v_cvt_pk_bf16_f32 v4, v4, v5\n"
  "v_cvt_pk_bf16_f32 v5, v6, v7\n"
  "v_cvt_pk_bf16_f32 v8, v8, v9\n"
  "v_cvt_pk_bf16_f32 v9, v10, v11\n"
  "v_cvt_pk_bf16_f32 v12, v12, v13\n"
  "v_cvt_pk_bf16_f32 v13, v14, v15\n"
  "v_cvt_pk_bf16_f32 v16, v16, v17\n"
  "v_cvt_pk_bf16_f32 v17, v18, v19\n"
  "v_cvt_pk_bf16_f32 v20, v20, v21\n"
  "v_cvt_pk_bf16_f32 v21, v22, v23\n"
  "v_cvt_pk_bf16_f32 v24, v24, v25\n"
  "v_cvt_pk_bf16_f32 v25, v26, v27\n"
  "v_cvt_pk_bf16_f32 v28, v28, v29\n"
  "v_cvt_pk_bf16_f32 v29, v30, v31\n"
  "ds_write_b64 v128, v[0:1] offset:0\n"
  "ds_write_b64 v129, v[4:5] offset:0\n"
  "ds_write_b64 v130, v[8:9] offset:0\n"
  "ds_write_b64 v131, v[12:13] offset:0\n"
  "ds_write_b64 v128, v[16:17] offset:128\n"
  "ds_write_b64 v129, v[20:21] offset:128\n"
  "ds_write_b64 v130, v[24:25] offset:128\n"
  "ds_write_b64 v131, v[28:29] offset:128\n"
  "v_pk_mul_f32 v[32:33], v[32:33], v[198:199] op_sel_hi:[1,0]\n"
  "v_pk_mul_f32 v[34:35], v[34:35], v[198:199] op_sel_hi:[1,0]\n"
  "v_pk_mul_f32 v[36:37], v[36:37], v[198:199] op_sel_hi:[1,0]\n"
  "v_pk_mul_f32 v[38:39], v[38:39], v[198:199] op_sel_hi:[1,0]\n"
  "v_pk_mul_f32 v[40:41], v[40:41], v[198:199] op_sel_hi:[1,0]\n"
  "v_pk_mul_f32 v[42:43], v[42:43], v[198:199] op_sel_hi:[1,0]\n"
  "v_pk_mul_f32 v[44:45], v[44:45], v[198:199] op_sel_hi:[1,0]\n"
  "v_pk_mul_f32 v[46:47], v[46:47], v[198:199] op_sel_hi:[1,0]\n"
  "v_pk_mul_f32 v[48:49], v[48:49], v[198:199] op_sel_hi:[1,0]\n"
  "v_pk_mul_f32 v[50:51], v[50:51], v[198:199] op_sel_hi:[1,0]\n"
  "v_pk_mul_f32 v[52:53], v[52:53], v[198:199] op_sel_hi:[1,0]\n"
  "v_pk_mul_f32 v[54:55], v[54:55], v[198:199] op_sel_hi:[1,0]\n"
  "v_pk_mul_f32 v[56:57], v[56:57], v[198:199] op_sel_hi:[1,0]\n"
  "v_pk_mul_f32 v[58:59], v[58:59], v[198:199] op_sel_hi:[1,0]\n"
  "v_pk_mul_f32 v[60:61], v[60:61], v[198:199] op_sel_hi:[1,0]\n"
  "v_pk_mul_f32 v[62:63], v[62:63], v[198:199] op_sel_hi:[1,0]\n"
  "v_cvt_pk_bf16_f32 v32, v32, v33\n"
  "v_cvt_pk_bf16_f32 v33, v34, v35\n"
  "v_cvt_pk_bf16_f32 v36, v36, v37\n"
  "v_cvt_pk_bf16_f32 v37, v38, v39\n"
  "s_waitcnt lgkmcnt(0)\n"
  "ds_read_b128 v[0:3], v132 offset:0\n"
  "ds_read_b128 v[4:7], v133 offset:1024\n"
  "ds_read_b128 v[8:11], v132 offset:2048\n"
  "ds_read_b128 v[12:15], v133 offset:3072\n"
  "v_cvt_pk_bf16_f32 v40, v40, v41\n"
  "v_cvt_pk_bf16_f32 v41, v42, v43\n"
  "v_cvt_pk_bf16_f32 v44, v44, v45\n"
  "v_cvt_pk_bf16_f32 v45, v46, v47\n"
  "v_cvt_pk_bf16_f32 v48, v48, v49\n"
  "v_cvt_pk_bf16_f32 v49, v50, v51\n"
  "v_cvt_pk_bf16_f32 v52, v52, v53\n"
  "v_cvt_pk_bf16_f32 v53, v54, v55\n"
  "v_cvt_pk_bf16_f32 v56, v56, v57\n"
  "v_cvt_pk_bf16_f32 v57, v58, v59\n"
  "v_cvt_pk_bf16_f32 v60, v60, v61\n"
  "v_cvt_pk_bf16_f32 v61, v62, v63\n"
  "ds_write_b64 v128, v[32:33] offset:4096\n"
  "ds_write_b64 v129, v[36:37] offset:4096\n"
  "ds_write_b64 v130, v[40:41] offset:4096\n"
  "ds_write_b64 v131, v[44:45] offset:4096\n"
  "ds_write_b64 v128, v[48:49] offset:4224\n"
  "ds_write_b64 v129, v[52:53] offset:4224\n"
  "ds_write_b64 v130, v[56:57] offset:4224\n"
  "ds_write_b64 v131, v[60:61] offset:4224\n"
  "s_waitcnt lgkmcnt(8)\n"
  "v_mfma_f32_16x16x32_bf16 v[200:203], v[216:219], v[176:179], v[200:203]\n"
  "buffer_store_dwordx4 v[0:3], v229, s[20:23], s34 offen sc1 nt\n"
  "v_mfma_f32_16x16x32_bf16 v[64:67], a[224:227], v[176:179], v[64:67]\n"
  "v_mfma_f32_16x16x32_bf16 v[68:71], a[228:231], v[176:179], v[68:71]\n"
  "v_mfma_f32_16x16x32_bf16 v[72:75], a[232:235], v[176:179], v[72:75]\n"
  "s_add_u32 s34, s34, 0x4000\n"
  "v_mfma_f32_16x16x32_bf16 v[76:79], a[236:239], v[176:179], v[76:79]\n"
  "buffer_store_dwordx4 v[4:7], v229, s[20:23], s34 offen sc1 nt\n"
  "v_mfma_f32_16x16x32_bf16 v[80:83], a[240:243], v[176:179], v[80:83]\n"
  "v_mfma_f32_16x16x32_bf16 v[84:87], a[244:247], v[176:179], v[84:87]\n"
  "v_mfma_f32_16x16x32_bf16 v[88:91], a[248:251], v[176:179], v[88:91]\n"
  "s_waitcnt lgkmcnt(0)\n"
  "ds_read_b128 v[32:35], v132 offset:4096\n"
  "ds_read_b128 v[36:39], v133 offset:5120\n"
  "ds_read_b128 v[40:43], v132 offset:6144\n"
  "ds_read_b128 v[44:47], v133 offset:7168\n"
  "s_add_u32 s34, s34, 0x4000\n"
  "v_mfma_f32_16x16x32_bf16 v[92:95], a[252:255], v[176:179], v[92:95]\n"
  "buffer_store_dwordx4 v[8:11], v229, s[20:23], s34 offen sc1 nt\n"
  "v_med3_f32 v201, v200, s88, v234\n"
  "v_mfma_f32_16x16x32_bf16 v[204:207], v[216:219], v[184:187], v[204:207]\n"
  "v_rcp_f32 v202, v200\n"
  "v_mfma_f32_16x16x32_bf16 v[96:99], a[224:227], v[184:187], v[96:99]\n"
  "v_cmp_neq_f32 s[66:67], v201, v200\n"
  "v_mfma_f32_16x16x32_bf16 v[100:103], a[228:231], v[184:187], v[100:103]\n"
  "s_add_u32 s34, s34, 0x4000\n"
  "v_mfma_f32_16x16x32_bf16 v[104:107], a[232:235], v[184:187], v[104:107]\n"
  "buffer_store_dwordx4 v[12:15], v229, s[20:23], s34 offen sc1 nt\n"
  "v_mfma_f32_16x16x32_bf16 v[108:111], a[236:239], v[184:187], v[108:111]\n"
  "v_mfma_f32_16x16x32_bf16 v[112:115], a[240:243], v[184:187], v[112:115]\n"
  "v_mfma_f32_16x16x32_bf16 v[116:119], a[244:247], v[184:187], v[116:119]\n"
  "v_mfma_f32_16x16x32_bf16 v[120:123], a[248:251], v[184:187], v[120:123]\n"
  "v_mfma_f32_16x16x32_bf16 v[124:127], a[252:255], v[184:187], v[124:127]\n"
  "v_med3_f32 v205, v204, s88, v234\n"
  "v_rcp_f32 v206, v204\n"
  "v_cmp_neq_f32 s[68:69], v205, v204\n"
  "s_cmp_ge_u32 s35, 2146435072\n"
  "s_cselect_b64 s[64:65], 0, s[64:65]\n"
  "s_or_b64 s[62:63], s[62:63], s[64:65]\n"
  "s_cmp_ge_u32 s36, 2146435072\n"
  "s_cselect_b64 s[66:67], 0, s[66:67]\n"
  "s_or_b64 s[62:63], s[62:63], s[66:67]\n"
  "s_cmp_ge_u32 s37, 2146435072\n"
  "s_cselect_b64 s[68:69], 0, s[68:69]\n"
  "s_or_b64 s[62:63], s[62:63], s[68:69]\n"
  "s_cselect_b32 s58, 1, 0\n"
  "s_lshl_b32 s58, s58, s80\n"
  "s_or_b32 s54, s54, s58\n"
  "s_waitcnt lgkmcnt(0)\n"
  "v_pk_mul_f32 v[64:65], v[64:65], v[202:203] op_sel_hi:[1,0]\n"
  "v_pk_mul_f32 v[66:67], v[66:67], v[202:203] op_sel_hi:[1,0]\n"
  "v_pk_mul_f32 v[68:69], v[68:69], v[202:203] op_sel_hi:[1,0]\n"
  "v_pk_mul_f32 v[70:71], v[70:71], v[202:203] op_sel_hi:[1,0]\n"
  "v_pk_mul_f32 v[72:73], v[72:73], v[202:203] op_sel_hi:[1,0]\n"
  "v_pk_mul_f32 v[74:75], v[74:75], v[202:203] op_sel_hi:[1,0]\n"
  "v_pk_mul_f32 v[76:77], v[76:77], v[202:203] op_sel_hi:[1,0]\n"
  "v_pk_mul_f32 v[78:79], v[78:79], v[202:203] op_sel_hi:[1,0]\n"
  "buffer_store_dwordx4 v[32:35], v229, s[20:23], s35 offen sc1 nt\n"
  "v_pk_mul_f32 v[80:81], v[80:81], v[202:203] op_sel_hi:[1,0]\n"
  "v_pk_mul_f32 v[82:83], v[82:83], v[202:203] op_sel_hi:[1,0]\n"
  "v_pk_mul_f32 v[84:85], v[84:85], v[202:203] op_sel_hi:[1,0]\n"
  "v_pk_mul_f32 v[86:87], v[86:87], v[202:203] op_sel_hi:[1,0]\n"
  "v_pk_mul_f32 v[88:89], v[88:89], v[202:203] op_sel_hi:[1,0]\n"
  "v_pk_mul_f32 v[90:91], v[90:91], v[202:203] op_sel_hi:[1,0]\n"
  "v_pk_mul_f32 v[92:93], v[92:93], v[202:203] op_sel_hi:[1,0]\n"
  "v_pk_mul_f32 v[94:95], v[94:95], v[202:203] op_sel_hi:[1,0]\n"
  "s_add_u32 s35, s35, 0x4000\n"
  "buffer_store_dwordx4 v[36:39], v229, s[20:23], s35 offen sc1 nt\n"
  "v_cvt_pk_bf16_f32 v64, v64, v65\n"
  "v_cvt_pk_bf16_f32 v65, v66, v67\n"
  "v_cvt_pk_bf16_f32 v68, v68, v69\n"
  "v_cvt_pk_bf16_f32 v69, v70, v71\n"
  "v_cvt_pk_bf16_f32 v72, v72, v73\n"
  "v_cvt_pk_bf16_f32 v73, v74, v75\n"
  "v_cvt_pk_bf16_f32 v76, v76, v77\n"
  "v_cvt_pk_bf16_f32 v77, v78, v79\n"
  "s_add_u32 s35, s35, 0x4000\n"
  "buffer_store_dwordx4 v[40:43], v229, s[20:23], s35 offen sc1 nt\n"
  "v_cvt_pk_bf16_f32 v80, v80, v81\n"
  "v_cvt_pk_bf16_f32 v81, v82, v83\n"
  "v_cvt_pk_bf16_f32 v84, v84, v85\n"
  "v_cvt_pk_bf16_f32 v85, v86, v87\n"
  "v_cvt_pk_bf16_f32 v88, v88, v89\n"
  "v_cvt_pk_bf16_f32 v89, v90, v91\n"
  "v_cvt_pk_bf16_f32 v92, v92, v93\n"
  "v_cvt_pk_bf16_f32 v93, v94, v95\n"
  "s_add_u32 s35, s35, 0x4000\n"
  "buffer_store_dwordx4 v[44:47], v229, s[20:23], s35 offen sc1 nt\n"
  "ds_write_b64 v128, v[64:65] offset:8192\n"
  "ds_write_b64 v129, v[68:69] offset:8192\n"
  "ds_write_b64 v130, v[72:73] offset:8192\n"
  "ds_write_b64 v131, v[76:77] offset:8192\n"
  "ds_write_b64 v128, v[80:81] offset:8320\n"
  "ds_write_b64 v129, v[84:85] offset:8320\n"
  "ds_write_b64 v130, v[88:89] offset:8320\n"
  "ds_write_b64 v131, v[92:93] offset:8320\n"
  "s_cmp_eq_u32 s78, 3\n"
  "s_cbranch_scc1 L_Zf3\n"
  "v_pk_mul_f32 v[96:97], v[96:97], v[206:207] op_sel_hi:[1,0]\n"
  "v_pk_mul_f32 v[98:99], v[98:99], v[206:207] op_sel_hi:[1,0]\n"
  "v_pk_mul_f32 v[100:101], v[100:101], v[206:207] op_sel_hi:[1,0]\n"
  "v_pk_mul_f32 v[102:103], v[102:103], v[206:207] op_sel_hi:[1,0]\n"
  "v_pk_mul_f32 v[104:105], v[104:105], v[206:207] op_sel_hi:[1,0]\n"
  "v_pk_mul_f32 v[106:107], v[106:107], v[206:207] op_sel_hi:[1,0]\n"
  "v_pk_mul_f32 v[108:109], v[108:109], v[206:207] op_sel_hi:[1,0]\n"
  "v_pk_mul_f32 v[110:111], v[110:111], v[206:207] op_sel_hi:[1,0]\n"
  "v_pk_mul_f32 v[112:113], v[112:113], v[206:207] op_sel_hi:[1,0]\n"
  "v_pk_mul_f32 v[114:115], v[114:115], v[206:207] op_sel_hi:[1,0]\n"
  "v_pk_mul_f32 v[116:117], v[116:117], v[206:207] op_sel_hi:[1,0]\n"
  "v_pk_mul_f32 v[118:119], v[118:119], v[206:207] op_sel_hi:[1,0]\n"
  "v_pk_mul_f32 v[120:121], v[120:121], v[206:207] op_sel_hi:[1,0]\n"
  "v_pk_mul_f32 v[122:123], v[122:123], v[206:207] op_sel_hi:[1,0]\n"
  "v_pk_mul_f32 v[124:125], v[124:125], v[206:207] op_sel_hi:[1,0]\n"
  "v_pk_mul_f32 v[126:127], v[126:127], v[206:207] op_sel_hi:[1,0]\n"
  "v_cvt_pk_bf16_f32 v96, v96, v97\n"
  "v_cvt_pk_bf16_f32 v97, v98, v99\n"
  "v_cvt_pk_bf16_f32 v100, v100, v101\n"
  "v_cvt_pk_bf16_f32 v101, v102, v103\n"
  "s_waitcnt lgkmcnt(0)\n"
  "ds_read_b128 v[64:67], v132 offset:8192\n"
  "ds_read_b128 v[68:71], v133 offset:9216\n"
  "ds_read_b128 v[72:75], v132 offset:10240\n"
  "ds_read_b128 v[76:79], v133 offset:11264\n"
  "v_cvt_pk_bf16_f32 v104, v104, v105\n"
  "v_cvt_pk_bf16_f32 v105, v106, v107\n"
  "v_cvt_pk_bf16_f32 v108, v108, v109\n"
  "v_cvt_pk_bf16_f32 v109, v110, v111\n"
  "v_cvt_pk_bf16_f32 v112, v112, v113\n"
  "v_cvt_pk_bf16_f32 v113, v114, v115\n"
  "v_cvt_pk_bf16_f32 v116, v116, v117\n"
  "v_cvt_pk_bf16_f32 v117, v118, v119\n"
  "v_cvt_pk_bf16_f32 v120, v120, v121\n"
  "v_cvt_pk_bf16_f32 v121, v122, v123\n"
  "v_cvt_pk_bf16_f32 v124, v124, v125\n"
  "v_cvt_pk_bf16_f32 v125, v126, v127\n"
  "ds_write_b64 v128, v[96:97] offset:12288\n"
  "ds_write_b64 v129, v[100:101] offset:12288\n"
  "ds_write_b64 v130, v[104:105] offset:12288\n"
  "ds_write_b64 v131, v[108:109] offset:12288\n"
  "ds_write_b64 v128, v[112:113] offset:12416\n"
  "ds_write_b64 v129, v[116:117] offset:12416\n"
  "ds_write_b64 v130, v[120:121] offset:12416\n"
  "ds_write_b64 v131, v[124:125] offset:12416\n"
  "s_waitcnt lgkmcnt(8)\n"
  "buffer_store_dwordx4 v[64:67], v229, s[20:23], s36 offen sc1 nt\n"
  "s_add_u32 s36, s36, 0x4000\n"
  "buffer_store_dwordx4 v[68:71], v229, s[20:23], s36 offen sc1 nt\n"
  "s_waitcnt lgkmcnt(0)\n"
  "ds_read_b128 v[96:99], v132 offset:12288\n"
  "ds_read_b128 v[100:103], v133 offset:13312\n"
  "ds_read_b128 v[104:107], v132 offset:14336\n"
  "ds_read_b128 v[108:111], v133 offset:15360\n"
  "s_add_u32 s36, s36, 0x4000\n"
  "buffer_store_dwordx4 v[72:75], v229, s[20:23], s36 offen sc1 nt\n"
  "s_add_u32 s36, s36, 0x4000\n"
  "buffer_store_dwordx4 v[76:79], v229, s[20:23], s36 offen sc1 nt\n"
  "s_waitcnt lgkmcnt(0)\n"
  "buffer_store_dwordx4 v[96:99], v229, s[20:23], s37 offen sc1 nt\n"
  "s_add_u32 s37, s37, 0x4000\n"
  "buffer_store_dwordx4 v[100:103], v229, s[20:23], s37 offen sc1 nt\n"
  "s_add_u32 s37, s37, 0x4000\n"
  "buffer_store_dwordx4 v[104:107], v229, s[20:23], s37 offen sc1 nt\n"
  "s_add_u32 s37, s37, 0x4000\n"
  "buffer_store_dwordx4 v[108:111], v229, s[20:23], s37 offen sc1 nt\n"
  "s_branch L_exit\n"
  "L_Zf1:\n"
  "v_bfe_u32 v235, v226, 6, 2\n"
  "v_lshrrev_b32 v250, 8, v226\n"
  "v_cmp_eq_u32 s[66:67], v235, v250\n"
  "v_and_b32 v235, 16, v226\n"
  "v_mov_b32 v250, 0x3f80\n"
  "v_lshlrev_b32 v235, v235, v250\n"
  "v_cndmask_b32 v235, v236, v235, s[66:67]\n"
  "v_and_b32 v250, 32, v226\n"
  "v_cmp_eq_u32 s[68:69], 0, v250\n"
  "v_mov_b32 v250, 0\n"
  "v_mov_b32 v251, 0\n"
  "v_mov_b32 v252, 0\n"
  "v_mov_b32 v253, 0\n"
  "v_cndmask_b32 v248, v236, v235, s[68:69]\n"
  "v_cndmask_b32 v249, v235, v236, s[68:69]\n"
  "v_mov_b32 v254, v248\n"
  "v_mov_b32 v255, v249\n"
  "s_waitcnt vmcnt(1)\n"
  "s_waitcnt lgkmcnt(0)\n"
  "v_mfma_f32_16x16x32_bf16 v[192:195], v[216:219], v[160:163], v[192:195]\n"
  "v_fma_f32 v176, v176, s38, v210\n"
  "v_fma_f32 v177, v177, s38, v210\n"
  "v_mfma_f32_16x16x32_bf16 v[0:3], a[224:227], v[160:163], v[0:3]\n"
  "v_exp_f32 v176, v176\n"
  "v_mfma_f32_16x16x32_bf16 v[4:7], a[228:231], v[160:163], v[4:7]\n"
  "v_exp_f32 v177, v177\n"
  "v_mfma_f32_16x16x32_bf16 v[8:11], a[232:235], v[160:163], v[8:11]\n"
  "v_cvt_pk_bf16_f32 v176, v176, v177\n"
  "v_fma_f32 v178, v178, s38, v210\n"
  "v_mfma_f32_16x16x32_bf16 v[12:15], a[236:239], v[160:163], v[12:15]\n"
  "v_exp_f32 v178, v178\n"
  "v_mfma_f32_16x16x32_bf16 v[16:19], a[240:243], v[160:163], v[16:19]\n"
  "v_fma_f32 v179, v179, s38, v210\n"
  "v_fma_f32 v180, v180, s38, v210\n"
  "v_mfma_f32_16x16x32_bf16 v[20:23], a[244:247], v[160:163], v[20:23]\n"
  "v_exp_f32 v179, v179\n"
  "v_mfma_f32_16x16x32_bf16 v[24:27], a[248:251], v[160:163], v[24:27]\n"
  "v_cvt_pk_bf16_f32 v177, v178, v179\n"
  "v_fma_f32 v181, v181, s38, v210\n"
  "v_mfma_f32_16x16x32_bf16 v[28:31], a[252:255], v[160:163], v[28:31]\n"
  "v_exp_f32 v180, v180\n"
  "v_mfma_f32_16x16x32_bf16 v[196:199], v[216:219], v[168:171], v[196:199]\n"
  "v_exp_f32 v181, v181\n"
  "v_mfma_f32_16x16x32_bf16 v[32:35], a[224:227], v[168:171], v[32:35]\n"
  "v_cvt_pk_bf16_f32 v178, v180, v181\n"
  "v_fma_f32 v182, v182, s38, v210\n"
  "v_mfma_f32_16x16x32_bf16 v[36:39], a[228:231], v[168:171], v[36:39]\n"
  "v_exp_f32 v182, v182\n"
  "v_mfma_f32_16x16x32_bf16 v[40:43], a[232:235], v[168:171], v[40:43]\n"
  "v_fma_f32 v183, v183, s38, v210\n"
  "v_mfma_f32_16x16x32_bf16 v[44:47], a[236:239], v[168:171], v[44:47]\n"
  "v_exp_f32 v183, v183\n"
  "v_mfma_f32_16x16x32_bf16 v[48:51], a[240:243], v[168:171], v[48:51]\n"
  "v_cvt_pk_bf16_f32 v179, v182, v183\n"
  "v_mfma_f32_16x16x32_bf16 v[52:55], a[244:247], v[168:171], v[52:55]\n"
  "v_mfma_f32_16x16x32_bf16 v[56:59], a[248:251], v[168:171], v[56:59]\n"
  "v_mfma_f32_16x16x32_bf16 v[60:63], a[252:255], v[168:171], v[60:63]\n"
  "s_cmp_eq_u32 s75, 0\n"
  "s_cbranch_scc1 L_Zf1a\n"
  "v_mfma_f32_16x16x32_bf16 v[0:3], v[248:251], a[128:131], v[0:3]\n"
  "v_mfma_f32_16x16x32_bf16 v[4:7], v[252:255], a[128:131], v[4:7]\n"
  "v_mfma_f32_16x16x32_bf16 v[8:11], v[248:251], a[132:135], v[8:11]\n"
  "v_mfma_f32_16x16x32_bf16 v[12:15], v[252:255], a[132:135], v[12:15]\n"
  "v_mfma_f32_16x16x32_bf16 v[16:19], v[248:251], a[136:139], v[16:19]\n"
  "v_mfma_f32_16x16x32_bf16 v[20:23], v[252:255], a[136:139], v[20:23]\n"
  "v_mfma_f32_16x16x32_bf16 v[24:27], v[248:251], a[140:143], v[24:27]\n"
  "v_mfma_f32_16x16x32_bf16 v[28:31], v[252:255], a[140:143], v[28:31]\n"
  "v_mfma_f32_16x16x32_bf16 v[32:35], v[248:251], a[144:147], v[32:35]\n"
  "v_mfma_f32_16x16x32_bf16 v[36:39], v[252:255], a[144:147], v[36:39]\n"
  "v_mfma_f32_16x16x32_bf16 v[40:43], v[248:251], a[148:151], v[40:43]\n"
  "v_mfma_f32_16x16x32_bf16 v[44:47], v[252:255], a[148:151], v[44:47]\n"
  "v_mfma_f32_16x16x32_bf16 v[48:51], v[248:251], a[152:155], v[48:51]\n"
  "v_mfma_f32_16x16x32_bf16 v[52:55], v[252:255], a[152:155], v[52:55]\n"
  "v_mfma_f32_16x16x32_bf16 v[56:59], v[248:251], a[156:159], v[56:59]\n"
  "v_mfma_f32_16x16x32_bf16 v[60:63], v[252:255], a[156:159], v[60:63]\n"
  "v_add_f32 v192, v192, v244\n"
  "v_add_f32 v196, v196, v245\n"
  "L_Zf1a:\n"
  "v_mfma_f32_16x16x32_bf16 v[0:3], v[248:251], a[64:67], v[0:3]\n"
  "v_fma_f32 v184, v184, s38, v211\n"
  "v_fma_f32 v185, v185, s38, v211\n"
  "v_mfma_f32_16x16x32_bf16 v[4:7], v[252:255], a[64:67], v[4:7]\n"
  "v_exp_f32 v184, v184\n"
  "v_mfma_f32_16x16x32_bf16 v[8:11], v[248:251], a[68:71], v[8:11]\n"
  "v_exp_f32 v185, v185\n"
  "v_mfma_f32_16x16x32_bf16 v[12:15], v[252:255], a[68:71], v[12:15]\n"
  "v_cvt_pk_bf16_f32 v184, v184, v185\n"
  "v_fma_f32 v186, v186, s38, v211\n"
  "v_mfma_f32_16x16x32_bf16 v[16:19], v[248:251], a[72:75], v[16:19]\n"
  "v_exp_f32 v186, v186\n"
  "v_mfma_f32_16x16x32_bf16 v[20:23], v[252:255], a[72:75], v[20:23]\n"
  "v_fma_f32 v187, v187, s38, v211\n"
  "v_fma_f32 v188, v188, s38, v211\n"
  "v_mfma_f32_16x16x32_bf16 v[24:27], v[248:251], a[76:79], v[24:27]\n"
  "v_exp_f32 v187, v187\n"
  "v_mfma_f32_16x16x32_bf16 v[28:31], v[252:255], a[76:79], v[28:31]\n"
  "v_cvt_pk_bf16_f32 v185, v186, v187\n"
  "v_fma_f32 v189, v189, s38, v211\n"
  "v_mfma_f32_16x16x32_bf16 v[32:35], v[248:251], a[80:83], v[32:35]\n"
  "v_exp_f32 v188, v188\n"
  "v_mfma_f32_16x16x32_bf16 v[36:39], v[252:255], a[80:83], v[36:39]\n"
  "v_exp_f32 v189, v189\n"
  "v_mfma_f32_16x16x32_bf16 v[40:43], v[248:251], a[84:87], v[40:43]\n"
  "v_cvt_pk_bf16_f32 v186, v188, v189\n"
  "v_fma_f32 v190, v190, s38, v211\n"
  "v_mfma_f32_16x16x32_bf16 v[44:47], v[252:255], a[84:87], v[44:47]\n"
  "v_exp_f32 v190, v190\n"
  "v_mfma_f32_16x16x32_bf16 v[48:51], v[248:251], a[88:91], v[48:51]\n"
  "v_fma_f32 v191, v191, s38, v211\n"
  "v_mfma_f32_16x16x32_bf16 v[52:55], v[252:255], a[88:91], v[52:55]\n"
  "v_exp_f32 v191, v191\n"
  "v_mfma_f32_16x16x32_bf16 v[56:59], v[248:251], a[92:95], v[56:59]\n"
  "v_cvt_pk_bf16_f32 v187, v190, v191\n"
  "v_mfma_f32_16x16x32_bf16 v[60:63], v[252:255], a[92:95], v[60:63]\n"
  "v_add_f32 v192, v192, v240\n"
  "v_add_f32 v196, v196, v241\n"
  "v_med3_f32 v193, v192, s88, v234\n"
  "v_rcp_f32 v194, v192\n"
  "v_cmp_neq_f32 s[62:63], v193, v192\n"
  "v_med3_f32 v197, v196, s88, v234\n"
  "v_rcp_f32 v198, v196\n"
  "v_cmp_neq_f32 s[64:65], v197, v196\n"
  "v_pk_mul_f32 v[0:1], v[0:1], v[194:195] op_sel_hi:[1,0]\n"
  "v_pk_mul_f32 v[2:3], v[2:3], v[194:195] op_sel_hi:[1,0]\n"
  "v_pk_mul_f32 v[4:5], v[4:5], v[194:195] op_sel_hi:[1,0]\n"
  "v_pk_mul_f32 v[6:7], v[6:7], v[194:195] op_sel_hi:[1,0]\n"
  "v_pk_mul_f32 v[8:9], v[8:9], v[194:195] op_sel_hi:[1,0]\n"
  "v_pk_mul_f32 v[10:11], v[10:11], v[194:195] op_sel_hi:[1,0]\n"
  "v_pk_mul_f32 v[12:13], v[12:13], v[194:195] op_sel_hi:[1,0]\n"
  "v_pk_mul_f32 v[14:15], v[14:15], v[194:195] op_sel_hi:[1,0]\n"
  "v_pk_mul_f32 v[16:17], v[16:17], v[194:195] op_sel_hi:[1,0]\n"
  "v_pk_mul_f32 v[18:19], v[18:19], v[194:195] op_sel_hi:[1,0]\n"
  "v_pk_mul_f32 v[20:21], v[20:21], v[194:195] op_sel_hi:[1,0]\n"
  "v_pk_mul_f32 v[22:23], v[22:23], v[194:195] op_sel_hi:[1,0]\n"
  "v_pk_mul_f32 v[24:25], v[24:25], v[194:195] op_sel_hi:[1,0]\n"
  "v_pk_mul_f32 v[26:27], v[26:27], v[194:195] op_sel_hi:[1,0]\n"
  "v_pk_mul_f32 v[28:29], v[28:29], v[194:195] op_sel_hi:[1,0]\n"
  "v_pk_mul_f32 v[30:31], v[30:31], v[194:195] op_sel_hi:[1,0]\n"
  "v_cvt_pk_bf16_f32 v0, v0, v1\n"
  "v_cvt_pk_bf16_f32 v1, v2, v3\n"
  "v_cvt_pk_bf16_f32 v4, v4, v5\n"
  "v_cvt_pk_bf16_f32 v5, v6, v7\n"
  "v_cvt_pk_bf16_f32 v8, v8, v9\n"
  "v_cvt_pk_bf16_f32 v9, v10, v11\n"
  "v_cvt_pk_bf16_f32 v12, v12, v13\n"
  "v_cvt_pk_bf16_f32 v13, v14, v15\n"
  "v_cvt_pk_bf16_f32 v16, v16, v17\n"
  "v_cvt_pk_bf16_f32 v17, v18, v19\n"
  "v_cvt_pk_bf16_f32 v20, v20, v21\n"
  "v_cvt_pk_bf16_f32 v21, v22, v23\n"
  "v_cvt_pk_bf16_f32 v24, v24, v25\n"
  "v_cvt_pk_bf16_f32 v25, v26, v27\n"
  "v_cvt_pk_bf16_f32 v28, v28, v29\n"
  "v_cvt_pk_bf16_f32 v29, v30, v31\n"
  "ds_write_b64 v128, v[0:1] offset:0\n"
  "ds_write_b64 v129, v[4:5] offset:0\n"
  "ds_write_b64 v130, v[8:9] offset:0\n"
  "ds_write_b64 v131, v[12:13] offset:0\n"
  "ds_write_b64 v128, v[16:17] offset:128\n"
  "ds_write_b64 v129, v[20:21] offset:128\n"
  "ds_write_b64 v130, v[24:25] offset:128\n"
  "ds_write_b64 v131, v[28:29] offset:128\n"
  "v_pk_mul_f32 v[32:33], v[32:33], v[198:199] op_sel_hi:[1,0]\n"
  "v_pk_mul_f32 v[34:35], v[34:35], v[198:199] op_sel_hi:[1,0]\n"
  "v_pk_mul_f32 v[36:37], v[36:37], v[198:199] op_sel_hi:[1,0]\n"
  "v_pk_mul_f32 v[38:39], v[38:39], v[198:199] op_sel_hi:[1,0]\n"
  "v_pk_mul_f32 v[40:41], v[40:41], v[198:199] op_sel_hi:[1,0]\n"
  "v_pk_mul_f32 v[42:43], v[42:43], v[198:199] op_sel_hi:[1,0]\n"
  "v_pk_mul_f32 v[44:45], v[44:45], v[198:199] op_sel_hi:[1,0]\n"
  "v_pk_mul_f32 v[46:47], v[46:47], v[198:199] op_sel_hi:[1,0]\n"
  "v_pk_mul_f32 v[48:49], v[48:49], v[198:199] op_sel_hi:[1,0]\n"
  "v_pk_mul_f32 v[50:51], v[50:51], v[198:199] op_sel_hi:[1,0]\n"
  "v_pk_mul_f32 v[52:53], v[52:53], v[198:199] op_sel_hi:[1,0]\n"
  "v_pk_mul_f32 v[54:55], v[54:55], v[198:199] op_sel_hi:[1,0]\n"
  "v_pk_mul_f32 v[56:57], v[56:57], v[198:199] op_sel_hi:[1,0]\n"
  "v_pk_mul_f32 v[58:59], v[58:59], v[198:199] op_sel_hi:[1,0]\n"
  "v_pk_mul_f32 v[60:61], v[60:61], v[198:199] op_sel_hi:[1,0]\n"
  "v_pk_mul_f32 v[62:63], v[62:63], v[198:199] op_sel_hi:[1,0]\n"
  "v_cvt_pk_bf16_f32 v32, v32, v33\n"
  "v_cvt_pk_bf16_f32 v33, v34, v35\n"
  "v_cvt_pk_bf16_f32 v36, v36, v37\n"
  "v_cvt_pk_bf16_f32 v37, v38, v39\n"
  "s_waitcnt lgkmcnt(0)\n"
  "ds_read_b128 v[0:3], v132 offset:0\n"
  "ds_read_b128 v[4:7], v133 offset:1024\n"
  "ds_read_b128 v[8:11], v132 offset:2048\n"
  "ds_read_b128 v[12:15], v133 offset:3072\n"
  "v_cvt_pk_bf16_f32 v40, v40, v41\n"
  "v_cvt_pk_bf16_f32 v41, v42, v43\n"
  "v_cvt_pk_bf16_f32 v44, v44, v45\n"
  "v_cvt_pk_bf16_f32 v45, v46, v47\n"
  "v_cvt_pk_bf16_f32 v48, v48, v49\n"
  "v_cvt_pk_bf16_f32 v49, v50, v51\n"
  "v_cvt_pk_bf16_f32 v52, v52, v53\n"
  "v_cvt_pk_bf16_f32 v53, v54, v55\n"
  "v_cvt_pk_bf16_f32 v56, v56, v57\n"
  "v_cvt_pk_bf16_f32 v57, v58, v59\n"
  "v_cvt_pk_bf16_f32 v60, v60, v61\n"
  "v_cvt_pk_bf16_f32 v61, v62, v63\n"
  "ds_write_b64 v128, v[32:33] offset:4096\n"
  "ds_write_b64 v129, v[36:37] offset:4096\n"
  "ds_write_b64 v130, v[40:41] offset:4096\n"
  "ds_write_b64 v131, v[44:45] offset:4096\n"
  "ds_write_b64 v128, v[48:49] offset:4224\n"
  "ds_write_b64 v129, v[52:53] offset:4224\n"
  "ds_write_b64 v130, v[56:57] offset:4224\n"
  "ds_write_b64 v131, v[60:61] offset:4224\n"
  "s_waitcnt lgkmcnt(8)\n"
  "v_mfma_f32_16x16x32_bf16 v[200:203], v[216:219], v[176:179], v[200:203]\n"
  "buffer_store_dwordx4 v[0:3], v229, s[20:23], s34 offen sc1 nt\n"
  "v_mfma_f32_16x16x32_bf16 v[64:67], a[224:227], v[176:179], v[64:67]\n"
  "v_mfma_f32_16x16x32_bf16 v[68:71], a[228:231], v[176:179], v[68:71]\n"
  "v_mfma_f32_16x16x32_bf16 v[72:75], a[232:235], v[176:179], v[72:75]\n"
  "s_add_u32 s34, s34, 0x4000\n"
  "v_mfma_f32_16x16x32_bf16 v[76:79], a[236:239], v[176:179], v[76:79]\n"
  "buffer_store_dwordx4 v[4:7], v229, s[20:23], s34 offen sc1 nt\n"
  "v_mfma_f32_16x16x32_bf16 v[80:83], a[240:243], v[176:179], v[80:83]\n"
  "v_mfma_f32_16x16x32_bf16 v[84:87], a[244:247], v[176:179], v[84:87]\n"
  "v_mfma_f32_16x16x32_bf16 v[88:91], a[248:251], v[176:179], v[88:91]\n"
  "s_waitcnt lgkmcnt(0)\n"
  "ds_read_b128 v[32:35], v132 offset:4096\n"
  "ds_read_b128 v[36:39], v133 offset:5120\n"
  "ds_read_b128 v[40:43], v132 offset:6144\n"
  "ds_read_b128 v[44:47], v133 offset:7168\n"
  "s_add_u32 s34, s34, 0x4000\n"
  "v_mfma_f32_16x16x32_bf16 v[92:95], a[252:255], v[176:179], v[92:95]\n"
  "buffer_store_dwordx4 v[8:11], v229, s[20:23], s34 offen sc1 nt\n"
  "v_mfma_f32_16x16x32_bf16 v[204:207], v[216:219], v[184:187], v[204:207]\n"
  "v_mfma_f32_16x16x32_bf16 v[96:99], a[224:227], v[184:187], v[96:99]\n"
  "v_mfma_f32_16x16x32_bf16 v[100:103], a[228:231], v[184:187], v[100:103]\n"
  "s_add_u32 s34, s34, 0x4000\n"
  "v_mfma_f32_16x16x32_bf16 v[104:107], a[232:235], v[184:187], v[104:107]\n"
  "buffer_store_dwordx4 v[12:15], v229, s[20:23], s34 offen sc1 nt\n"
  "v_mfma_f32_16x16x32_bf16 v[108:111], a[236:239], v[184:187], v[108:111]\n"
  "v_mfma_f32_16x16x32_bf16 v[112:115], a[240:243], v[184:187], v[112:115]\n"
  "v_mfma_f32_16x16x32_bf16 v[116:119], a[244:247], v[184:187], v[116:119]\n"
  "v_mfma_f32_16x16x32_bf16 v[120:123], a[248:251], v[184:187], v[120:123]\n"
  "v_mfma_f32_16x16x32_bf16 v[124:127], a[252:255], v[184:187], v[124:127]\n"
  "s_cmp_eq_u32 s75, 0\n"
  "s_cbranch_scc1 L_Zf1b\n"
  "v_mfma_f32_16x16x32_bf16 v[64:67], v[248:251], a[160:163], v[64:67]\n"
  "v_mfma_f32_16x16x32_bf16 v[68:71], v[252:255], a[160:163], v[68:71]\n"
  "v_mfma_f32_16x16x32_bf16 v[72:75], v[248:251], a[164:167], v[72:75]\n"
  "v_mfma_f32_16x16x32_bf16 v[76:79], v[252:255], a[164:167], v[76:79]\n"
  "v_mfma_f32_16x16x32_bf16 v[80:83], v[248:251], a[168:171], v[80:83]\n"
  "v_mfma_f32_16x16x32_bf16 v[84:87], v[252:255], a[168:171], v[84:87]\n"
  "v_mfma_f32_16x16x32_bf16 v[88:91], v[248:251], a[172:175], v[88:91]\n"
  "v_mfma_f32_16x16x32_bf16 v[92:95], v[252:255], a[172:175], v[92:95]\n"
  "v_mfma_f32_16x16x32_bf16 v[96:99], v[248:251], a[176:179], v[96:99]\n"
  "v_mfma_f32_16x16x32_bf16 v[100:103], v[252:255], a[176:179], v[100:103]\n"
  "v_mfma_f32_16x16x32_bf16 v[104:107], v[248:251], a[180:183], v[104:107]\n"
  "v_mfma_f32_16x16x32_bf16 v[108:111], v[252:255], a[180:183], v[108:111]\n"
  "v_mfma_f32_16x16x32_bf16 v[112:115], v[248:251], a[184:187], v[112:115]\n"
  "v_mfma_f32_16x16x32_bf16 v[116:119], v[252:255], a[184:187], v[116:119]\n"
  "v_mfma_f32_16x16x32_bf16 v[120:123], v[248:251], a[188:191], v[120:123]\n"
  "v_mfma_f32_16x16x32_bf16 v[124:127], v[252:255], a[188:191], v[124:127]\n"
  "v_add_f32 v200, v200, v246\n"
  "v_add_f32 v204, v204, v247\n"
  "L_Zf1b:\n"
  "s_waitcnt lgkmcnt(0)\n"
  "v_mfma_f32_16x16x32_bf16 v[64:67], v[248:251], a[96:99], v[64:67]\n"
  "buffer_store_dwordx4 v[32:35], v229, s[20:23], s35 offen sc1 nt\n"
  "v_add_f32 v200, v200, v242\n"
  "v_mfma_f32_16x16x32_bf16 v[68:71], v[252:255], a[96:99], v[68:71]\n"
  "v_add_f32 v204, v204, v243\n"
  "v_med3_f32 v201, v200, s88, v234\n"
  "v_mfma_f32_16x16x32_bf16 v[72:75], v[248:251], a[100:103], v[72:75]\n"
  "v_rcp_f32 v202, v200\n"
  "v_mfma_f32_16x16x32_bf16 v[76:79], v[252:255], a[100:103], v[76:79]\n"
  "v_cmp_neq_f32 s[66:67], v201, v200\n"
  "v_med3_f32 v205, v204, s88, v234\n"
  "s_add_u32 s35, s35, 0x4000\n"
  "v_mfma_f32_16x16x32_bf16 v[80:83], v[248:251], a[104:107], v[80:83]\n"
  "buffer_store_dwordx4 v[36:39], v229, s[20:23], s35 offen sc1 nt\n"
  "v_mfma_f32_16x16x32_bf16 v[84:87], v[252:255], a[104:107], v[84:87]\n"
  "v_rcp_f32 v206, v204\n"
  "v_mfma_f32_16x16x32_bf16 v[88:91], v[248:251], a[108:111], v[88:91]\n"
  "v_cmp_neq_f32 s[68:69], v205, v204\n"
  "v_mfma_f32_16x16x32_bf16 v[92:95], v[252:255], a[108:111], v[92:95]\n"
  "s_add_u32 s35, s35, 0x4000\n"
  "v_mfma_f32_16x16x32_bf16 v[96:99], v[248:251], a[112:115], v[96:99]\n"
  "buffer_store_dwordx4 v[40:43], v229, s[20:23], s35 offen sc1 nt\n"
  "v_mfma_f32_16x16x32_bf16 v[100:103], v[252:255], a[112:115], v[100:103]\n"
  "v_mfma_f32_16x16x32_bf16 v[104:107], v[248:251], a[116:119], v[104:107]\n"
  "v_mfma_f32_16x16x32_bf16 v[108:111], v[252:255], a[116:119], v[108:111]\n"
  "s_add_u32 s35, s35, 0x4000\n"
  "v_mfma_f32_16x16x32_bf16 v[112:115], v[248:251], a[120:123], v[112:115]\n"
  "buffer_store_dwordx4 v[44:47], v229, s[20:23], s35 offen sc1 nt\n"
  "v_mfma_f32_16x16x32_bf16 v[116:119], v[252:255], a[120:123], v[116:119]\n"
  "v_mfma_f32_16x16x32_bf16 v[120:123], v[248:251], a[124:127], v[120:123]\n"
  "v_mfma_f32_16x16x32_bf16 v[124:127], v[252:255], a[124:127], v[124:127]\n"
  "s_cmp_ge_u32 s35, 2146435072\n"
  "s_cselect_b64 s[64:65], 0, s[64:65]\n"
  "s_or_b64 s[62:63], s[62:63], s[64:65]\n"
  "s_cmp_ge_u32 s36, 2146435072\n"
  "s_cselect_b64 s[66:67], 0, s[66:67]\n"
  "s_or_b64 s[62:63], s[62:63], s[66:67]\n"
  "s_cmp_ge_u32 s37, 2146435072\n"
  "s_cselect_b64 s[68:69], 0, s[68:69]\n"
  "s_or_b64 s[62:63], s[62:63], s[68:69]\n"
  "s_cselect_b32 s58, 1, 0\n"
  "s_lshl_b32 s58, s58, s80\n"
  "s_or_b32 s54, s54, s58\n"
  "v_pk_mul_f32 v[64:65], v[64:65], v[202:203] op_sel_hi:[1,0]\n"
  "v_pk_mul_f32 v[66:67], v[66:67], v[202:203] op_sel_hi:[1,0]\n"
  "v_pk_mul_f32 v[68:69], v[68:69], v[202:203] op_sel_hi:[1,0]\n"
  "v_pk_mul_f32 v[70:71], v[70:71], v[202:203] op_sel_hi:[1,0]\n"
  "v_pk_mul_f32 v[72:73], v[72:73], v[202:203] op_sel_hi:[1,0]\n"
  "v_pk_mul_f32 v[74:75], v[74:75], v[202:203] op_sel_hi:[1,0]\n"
  "v_pk_mul_f32 v[76:77], v[76:77], v[202:203] op_sel_hi:[1,0]\n"
  "v_pk_mul_f32 v[78:79], v[78:79], v[202:203] op_sel_hi:[1,0]\n"
  "v_pk_mul_f32 v[80:81], v[80:81], v[202:203] op_sel_hi:[1,0]\n"
  "v_pk_mul_f32 v[82:83], v[82:83], v[202:203] op_sel_hi:[1,0]\n"
  "v_pk_mul_f32 v[84:85], v[84:85], v[202:203] op_sel_hi:[1,0]\n"
  "v_pk_mul_f32 v[86:87], v[86:87], v[202:203] op_sel_hi:[1,0]\n"
  "v_pk_mul_f32 v[88:89], v[88:89], v[202:203] op_sel_hi:[1,0]\n"
  "v_pk_mul_f32 v[90:91], v[90:91], v[202:203] op_sel_hi:[1,0]\n"
  "v_pk_mul_f32 v[92:93], v[92:93], v[202:203] op_sel_hi:[1,0]\n"
  "v_pk_mul_f32 v[94:95], v[94:95], v[202:203] op_sel_hi:[1,0]\n"
  "v_cvt_pk_bf16_f32 v64, v64, v65\n"
  "v_cvt_pk_bf16_f32 v65, v66, v67\n"
  "v_cvt_pk_bf16_f32 v68, v68, v69\n"
  "v_cvt_pk_bf16_f32 v69, v70, v71\n"
  "v_cvt_pk_bf16_f32 v72, v72, v73\n"
  "v_cvt_pk_bf16_f32 v73, v74, v75\n"
  "v_cvt_pk_bf16_f32 v76, v76, v77\n"
  "v_cvt_pk_bf16_f32 v77, v78, v79\n"
  "v_cvt_pk_bf16_f32 v80, v80, v81\n"
  "v_cvt_pk_bf16_f32 v81, v82, v83\n"
  "v_cvt_pk_bf16_f32 v84, v84, v85\n"
  "v_cvt_pk_bf16_f32 v85, v86, v87\n"
  "v_cvt_pk_bf16_f32 v88, v88, v89\n"
  "v_cvt_pk_bf16_f32 v89, v90, v91\n"
  "v_cvt_pk_bf16_f32 v92, v92, v93\n"
  "v_cvt_pk_bf16_f32 v93, v94, v95\n"
  "ds_write_b64 v128, v[64:65] offset:8192\n"
  "ds_write_b64 v129, v[68:69] offset:8192\n"
  "ds_write_b64 v130, v[72:73] offset:8192\n"
  "ds_write_b64 v131, v[76:77] offset:8192\n"
  "ds_write_b64 v128, v[80:81] offset:8320\n"
  "ds_write_b64 v129, v[84:85] offset:8320\n"
  "ds_write_b64 v130, v[88:89] offset:8320\n"
  "ds_write_b64 v131, v[92:93] offset:8320\n"
  "s_cmp_eq_u32 s78, 3\n"
  "s_cbranch_scc1 L_Zf3\n"
  "v_pk_mul_f32 v[96:97], v[96:97], v[206:207] op_sel_hi:[1,0]\n"
  "v_pk_mul_f32 v[98:99], v[98:99], v[206:207] op_sel_hi:[1,0]\n"
  "v_pk_mul_f32 v[100:101], v[100:101], v[206:207] op_sel_hi:[1,0]\n"
  "v_pk_mul_f32 v[102:103], v[102:103], v[206:207] op_sel_hi:[1,0]\n"
  "v_pk_mul_f32 v[104:105], v[104:105], v[206:207] op_sel_hi:[1,0]\n"
  "v_pk_mul_f32 v[106:107], v[106:107], v[206:207] op_sel_hi:[1,0]\n"
  "v_pk_mul_f32 v[108:109], v[108:109], v[206:207] op_sel_hi:[1,0]\n"
  "v_pk_mul_f32 v[110:111], v[110:111], v[206:207] op_sel_hi:[1,0]\n"
  "v_pk_mul_f32 v[112:113], v[112:113], v[206:207] op_sel_hi:[1,0]\n"
  "v_pk_mul_f32 v[114:115], v[114:115], v[206:207] op_sel_hi:[1,0]\n"
  "v_pk_mul_f32 v[116:117], v[116:117], v[206:207] op_sel_hi:[1,0]\n"
  "v_pk_mul_f32 v[118:119], v[118:119], v[206:207] op_sel_hi:[1,0]\n"
  "v_pk_mul_f32 v[120:121], v[120:121], v[206:207] op_sel_hi:[1,0]\n"
  "v_pk_mul_f32 v[122:123], v[122:123], v[206:207] op_sel_hi:[1,0]\n"
  "v_pk_mul_f32 v[124:125], v[124:125], v[206:207] op_sel_hi:[1,0]\n"
  "v_pk_mul_f32 v[126:127], v[126:127], v[206:207] op_sel_hi:[1,0]\n"
  "v_cvt_pk_bf16_f32 v96, v96, v97\n"
  "v_cvt_pk_bf16_f32 v97, v98, v99\n"
  "v_cvt_pk_bf16_f32 v100, v100, v101\n"
  "v_cvt_pk_bf16_f32 v101, v102, v103\n"
  "s_waitcnt lgkmcnt(0)\n"
  "ds_read_b128 v[64:67], v132 offset:8192\n"
  "ds_read_b128 v[68:71], v133 offset:9216\n"
  "ds_read_b128 v[72:75], v132 offset:10240\n"
  "ds_read_b128 v[76:79], v133 offset:11264\n"
  "v_cvt_pk_bf16_f32 v104, v104, v105\n"
  "v_cvt_pk_bf16_f32 v105, v106, v107\n"
  "v_cvt_pk_bf16_f32 v108, v108, v109\n"
  "v_cvt_pk_bf16_f32 v109, v110, v111\n"
  "v_cvt_pk_bf16_f32 v112, v112, v113\n"
  "v_cvt_pk_bf16_f32 v113, v114, v115\n"
  "v_cvt_pk_bf16_f32 v116, v116, v117\n"
  "v_cvt_pk_bf16_f32 v117, v118, v119\n"
  "v_cvt_pk_bf16_f32 v120, v120, v121\n"
  "v_cvt_pk_bf16_f32 v121, v122, v123\n"
  "v_cvt_pk_bf16_f32 v124, v124, v125\n"
  "v_cvt_pk_bf16_f32 v125, v126, v127\n"
  "ds_write_b64 v128, v[96:97] offset:12288\n"
  "ds_write_b64 v129, v[100:101] offset:12288\n"
  "ds_write_b64 v130, v[104:105] offset:12288\n"
  "ds_write_b64 v131, v[108:109] offset:12288\n"
  "ds_write_b64 v128, v[112:113] offset:12416\n"
  "ds_write_b64 v129, v[116:117] offset:12416\n"
  "ds_write_b64 v130, v[120:121] offset:12416\n"
  "ds_write_b64 v131, v[124:125] offset:12416\n"
  "s_waitcnt lgkmcnt(8)\n"
  "buffer_store_dwordx4 v[64:67], v229, s[20:23], s36 offen sc1 nt\n"
  "s_add_u32 s36, s36, 0x4000\n"
  "buffer_store_dwordx4 v[68:71], v229, s[20:23], s36 offen sc1 nt\n"
  "s_waitcnt lgkmcnt(0)\n"
  "ds_read_b128 v[96:99], v132 offset:12288\n"
  "ds_read_b128 v[100:103], v133 offset:13312\n"
  "ds_read_b128 v[104:107], v132 offset:14336\n"
  "ds_read_b128 v[108:111], v133 offset:15360\n"
  "s_add_u32 s36, s36, 0x4000\n"
  "buffer_store_dwordx4 v[72:75], v229, s[20:23], s36 offen sc1 nt\n"
  "s_add_u32 s36, s36, 0x4000\n"
  "buffer_store_dwordx4 v[76:79], v229, s[20:23], s36 offen sc1 nt\n"
  "s_waitcnt lgkmcnt(0)\n"
  "buffer_store_dwordx4 v[96:99], v229, s[20:23], s37 offen sc1 nt\n"
  "s_add_u32 s37, s37, 0x4000\n"
  "buffer_store_dwordx4 v[100:103], v229, s[20:23], s37 offen sc1 nt\n"
  "s_add_u32 s37, s37, 0x4000\n"
  "buffer_store_dwordx4 v[104:107], v229, s[20:23], s37 offen sc1 nt\n"
  "s_add_u32 s37, s37, 0x4000\n"
  "buffer_store_dwordx4 v[108:111], v229, s[20:23], s37 offen sc1 nt\n"
  "s_branch L_exit\n"
  "L_Zf3:\n"
  "s_waitcnt lgkmcnt(0)\n"
  "ds_read_b128 v[64:67], v132 offset:8192\n"
  "ds_read_b128 v[68:71], v133 offset:9216\n"
  "ds_read_b128 v[72:75], v132 offset:10240\n"
  "ds_read_b128 v[76:79], v133 offset:11264\n"
  "s_waitcnt lgkmcnt(0)\n"
  "buffer_store_dwordx4 v[64:67], v229, s[20:23], s36 offen sc1 nt\n"
  "s_add_u32 s36, s36, 0x4000\n"
  "buffer_store_dwordx4 v[68:71], v229, s[20:23], s36 offen sc1 nt\n"
  "s_add_u32 s36, s36, 0x4000\n"
  "buffer_store_dwordx4 v[72:75], v229, s[20:23], s36 offen sc1 nt\n"
  "s_add_u32 s36, s36, 0x4000\n"
  "buffer_store_dwordx4 v[76:79], v229, s[20:23], s36 offen sc1 nt\n"
  "s_branch L_exit\n"
  "L_Z:\n"
  "s_barrier\n"
  "ds_read_b64_tr_b16 a[224:225], v221 offset:0\n"
  "ds_read_b64_tr_b16 a[226:227], v221 offset:4096\n"
  "ds_read_b64_tr_b16 a[228:229], v239 offset:0\n"
  "ds_read_b64_tr_b16 a[230:231], v239 offset:4096\n"
  "ds_read_b64_tr_b16 a[232:233], v221 offset:512\n"
  "ds_read_b64_tr_b16 a[234:235], v221 offset:4608\n"
  "ds_read_b64_tr_b16 a[236:237], v239 offset:512\n"
  "ds_read_b64_tr_b16 a[238:239], v239 offset:4608\n"
  "ds_read_b64_tr_b16 a[240:241], v221 offset:1024\n"
  "ds_read_b64_tr_b16 a[242:243], v221 offset:5120\n"
  "ds_read_b64_tr_b16 a[244:245], v239 offset:1024\n"
  "ds_read_b64_tr_b16 a[246:247], v239 offset:5120\n"
  "ds_read_b64_tr_b16 a[248:249], v221 offset:1536\n"
  "ds_read_b64_tr_b16 a[250:251], v221 offset:5632\n"
  "ds_read_b64_tr_b16 a[252:253], v239 offset:1536\n"
  "ds_read_b64_tr_b16 a[254:255], v239 offset:5632\n"
  "buffer_store_dwordx2 v[232:233], v227, s[24:27], s51 offen sc1\n"
  "s_cmp_lg_u32 s77, -1\n"
  "s_cbranch_scc1 L_Zg\n"
  "s_cmp_lt_u32 s78, 3\n"
  "s_cbranch_scc1 L_Zg\n"
  "s_cmp_eq_u32 s76, 1\n"
  "s_cbranch_scc1 L_Zf\n"
  "s_cmp_eq_u32 s74, 0\n"
  "s_cbranch_scc1 L_Zf\n"
  "L_Zg:\n"
  "v_readfirstlane_b32 s62, v230\n"
  "v_readfirstlane_b32 s63, v231\n"
  "s_nop 0\n"
  "s_cmp_eq_u64 s[62:63], s[56:57]\n"
  "s_cselect_b32 s61, s73, 2146435072\n"
  "s_add_u32 s60, s75, 1\n"
  "s_cmp_eq_u32 s60, s74\n"
  "s_cselect_b32 s61, s61, 2146435072\n"
  "s_mov_b32 s76, s61\n"
  "s_cmp_ge_u32 s72, 64\n"
  "s_cbranch_scc1 L_m1Z\n"
  "v_lshrrev_b32 v235, 8, v226\n"
  "s_mul_i32 s58, s72, 15\n"
  "s_mov_b32 s59, 0x3f803f80\n"
  "v_mul_u32_u24 v235, 60, v235\n"
  "v_sub_u32 v235, s58, v235\n"
  "v_subrev_u32 v216, 480, v235\n"
  "v_subrev_u32 v217, 510, v235\n"
  "v_subrev_u32 v218, 720, v235\n"
  "v_subrev_u32 v219, 750, v235\n"
  "v_med3_i32 v216, v216, 0, 30\n"
  "v_med3_i32 v217, v217, 0, 30\n"
  "v_med3_i32 v218, v218, 0, 30\n"
  "v_med3_i32 v219, v219, 0, 30\n"
  "v_bfe_u32 v216, s59, 0, v216\n"
  "v_bfe_u32 v217, s59, 0, v217\n"
  "v_bfe_u32 v218, s59, 0, v218\n"
  "v_bfe_u32 v219, s59, 0, v219\n"
  "L_m1Z:\n"
  "s_lshl_b32 s59, s53, 10\n"
  "s_add_u32 s59, s61, s59\n"
  "s_add_u32 s59, s59, 65536\n"
  "buffer_load_dwordx4 v[240:243], v226, s[24:27], s59 offen sc1\n"
  "s_add_u32 s61, s61, s81\n"
  "buffer_load_dwordx4 a[64:67], v226, s[24:27], s61 offen offset:0 sc1\n"
  "buffer_load_dwordx4 a[68:71], v226, s[24:27], s61 offen offset:1024 sc1\n"
  "buffer_load_dwordx4 a[72:75], v226, s[24:27], s61 offen offset:2048 sc1\n"
  "buffer_load_dwordx4 a[76:79], v226, s[24:27], s61 offen offset:3072 sc1\n"
  "v_fma_f32 v176, v176, s38, v210\n"
  "v_fma_f32 v177, v177, s38, v210\n"
  "v_exp_f32 v176, v176\n"
  "v_fma_f32 v178, v178, s38, v210\n"
  "v_exp_f32 v177, v177\n"
  "v_fma_f32 v179, v179, s38, v210\n"
  "v_exp_f32 v178, v178\n"
  "v_cvt_pk_bf16_f32 v176, v176, v177\n"
  "v_fma_f32 v180, v180, s38, v210\n"
  "v_exp_f32 v179, v179\n"
  "v_fma_f32 v181, v181, s38, v210\n"
  "v_exp_f32 v180, v180\n"
  "v_cvt_pk_bf16_f32 v177, v178, v179\n"
  "v_fma_f32 v182, v182, s38, v210\n"
  "v_exp_f32 v181, v181\n"
  "v_fma_f32 v183, v183, s38, v210\n"
  "s_waitcnt lgkmcnt(0)\n"
  "s_add_u32 s61, s61, 4096\n"
  "v_mfma_f32_16x16x32_bf16 v[192:195], v[216:219], v[160:163], v[192:195]\n"
  "buffer_load_dwordx4 a[80:83], v226, s[24:27], s61 offen offset:0 sc1\n"
  "v_mfma_f32_16x16x32_bf16 v[0:3], a[224:227], v[160:163], v[0:3]\n"
  "v_exp_f32 v182, v182\n"
  "v_mfma_f32_16x16x32_bf16 v[4:7], a[228:231], v[160:163], v[4:7]\n"
  "v_cvt_pk_bf16_f32 v178, v180, v181\n"
  "v_fma_f32 v184, v184, s38, v211\n"
  "v_mfma_f32_16x16x32_bf16 v[8:11], a[232:235], v[160:163], v[8:11]\n"
  "buffer_load_dwordx4 a[84:87], v226, s[24:27], s61 offen offset:1024 sc1\n"
  "v_mfma_f32_16x16x32_bf16 v[12:15], a[236:239], v[160:163], v[12:15]\n"
  "v_exp_f32 v183, v183\n"
  "v_mfma_f32_16x16x32_bf16 v[16:19], a[240:243], v[160:163], v[16:19]\n"
  "v_fma_f32 v185, v185, s38, v211\n"
  "v_mfma_f32_16x16x32_bf16 v[20:23], a[244:247], v[160:163], v[20:23]\n"
  "buffer_load_dwordx4 a[88:91], v226, s[24:27], s61 offen offset:2048 sc1\n"
  "v_mfma_f32_16x16x32_bf16 v[24:27], a[248:251], v[160:163], v[24:27]\n"
  "v_exp_f32 v184, v184\n"
  "v_mfma_f32_16x16x32_bf16 v[28:31], a[252:255], v[160:163], v[28:31]\n"
  "v_cvt_pk_bf16_f32 v179, v182, v183\n"
  "v_fma_f32 v186, v186, s38, v211\n"
  "v_mfma_f32_16x16x32_bf16 v[196:199], v[216:219], v[168:171], v[196:199]\n"
  "buffer_load_dwordx4 a[92:95], v226, s[24:27], s61 offen offset:3072 sc1\n"
  "v_mfma_f32_16x16x32_bf16 v[32:35], a[224:227], v[168:171], v[32:35]\n"
  "v_exp_f32 v185, v185\n"
  "v_mfma_f32_16x16x32_bf16 v[36:39], a[228:231], v[168:171], v[36:39]\n"
  "v_fma_f32 v187, v187, s38, v211\n"
  "s_add_u32 s61, s61, 4096\n"
  "v_mfma_f32_16x16x32_bf16 v[40:43], a[232:235], v[168:171], v[40:43]\n"
  "buffer_load_dwordx4 a[96:99], v226, s[24:27], s61 offen offset:0 sc1\n"
  "v_mfma_f32_16x16x32_bf16 v[44:47], a[236:239], v[168:171], v[44:47]\n"
  "v_exp_f32 v186, v186\n"
  "v_mfma_f32_16x16x32_bf16 v[48:51], a[240:243], v[168:171], v[48:51]\n"
  "v_cvt_pk_bf16_f32 v184, v184, v185\n"
  "v_fma_f32 v188, v188, s38, v211\n"
  "v_mfma_f32_16x16x32_bf16 v[52:55], a[244:247], v[168:171], v[52:55]\n"
  "buffer_load_dwordx4 a[100:103], v226, s[24:27], s61 offen offset:1024 sc1\n"
  "v_mfma_f32_16x16x32_bf16 v[56:59], a[248:251], v[168:171], v[56:59]\n"
  "v_exp_f32 v187, v187\n"
  "v_mfma_f32_16x16x32_bf16 v[60:63], a[252:255], v[168:171], v[60:63]\n"
  "v_fma_f32 v189, v189, s38, v211\n"
  "v_exp_f32 v188, v188\n"
  "v_cvt_pk_bf16_f32 v185, v186, v187\n"
  "v_fma_f32 v190, v190, s38, v211\n"
  "v_exp_f32 v189, v189\n"
  "v_fma_f32 v191, v191, s38, v211\n"
  "v_exp_f32 v190, v190\n"
  "v_cvt_pk_bf16_f32 v186, v188, v189\n"
  "v_exp_f32 v191, v191\n"
  "s_nop 0\n"
  "v_cvt_pk_bf16_f32 v187, v190, v191\n"
  "v_mfma_f32_16x16x32_bf16 v[200:203], v[216:219], v[176:179], v[200:203]\n"
  "buffer_load_dwordx4 a[104:107], v226, s[24:27], s61 offen offset:2048 sc1\n"
  "v_and_b32 v128, 0xf0, v226\n"
  "v_mfma_f32_16x16x32_bf16 v[64:67], a[224:227], v[176:179], v[64:67]\n"
  "v_and_b32 v134, 0x70, v226\n"
  "v_lshl_add_u32 v128, v128, 4, v134\n"
  "v_mfma_f32_16x16x32_bf16 v[68:71], a[228:231], v[176:179], v[68:71]\n"
  "v_lshrrev_b32 v134, 5, v226\n"
  "v_and_b32 v134, 0x18, v134\n"
  "v_mfma_f32_16x16x32_bf16 v[72:75], a[232:235], v[176:179], v[72:75]\n"
  "buffer_load_dwordx4 a[108:111], v226, s[24:27], s61 offen offset:3072 sc1\n"
  "v_xor_b32 v128, v128, v134\n"
  "v_mfma_f32_16x16x32_bf16 v[76:79], a[236:239], v[176:179], v[76:79]\n"
  "v_add_u32 v128, s82, v128\n"
  "v_xor_b32 v129, 32, v128\n"
  "v_mfma_f32_16x16x32_bf16 v[80:83], a[240:243], v[176:179], v[80:83]\n"
  "v_xor_b32 v130, 64, v128\n"
  "v_xor_b32 v131, 96, v128\n"
  "s_add_u32 s61, s61, 4096\n"
  "v_mfma_f32_16x16x32_bf16 v[84:87], a[244:247], v[176:179], v[84:87]\n"
  "buffer_load_dwordx4 a[112:115], v226, s[24:27], s61 offen offset:0 sc1\n"
  "v_lshrrev_b32 v134, 4, v226\n"
  "v_mfma_f32_16x16x32_bf16 v[88:91], a[248:251], v[176:179], v[88:91]\n"
  "v_and_b32 v134, 0x30, v134\n"
  "v_xor_b32 v132, v226, v134\n"
  "v_mfma_f32_16x16x32_bf16 v[92:95], a[252:255], v[176:179], v[92:95]\n"
  "v_add_u32 v132, s82, v132\n"
  "v_xor_b32 v133, 64, v132\n"
  "v_mfma_f32_16x16x32_bf16 v[204:207], v[216:219], v[184:187], v[204:207]\n"
  "buffer_load_dwordx4 a[116:119], v226, s[24:27], s61 offen offset:1024 sc1\n"
  "s_mov_b32 s88, 0x35800000\n"
  "v_mfma_f32_16x16x32_bf16 v[96:99], a[224:227], v[184:187], v[96:99]\n"
  "v_mov_b32 v234, 0x67800000\n"
  "v_bfe_u32 v235, v226, 6, 2\n"
  "v_mfma_f32_16x16x32_bf16 v[100:103], a[228:231], v[184:187], v[100:103]\n"
  "v_lshrrev_b32 v250, 8, v226\n"
  "v_cmp_eq_u32 s[66:67], v235, v250\n"
  "v_mfma_f32_16x16x32_bf16 v[104:107], a[232:235], v[184:187], v[104:107]\n"
  "buffer_load_dwordx4 a[120:123], v226, s[24:27], s61 offen offset:2048 sc1\n"
  "v_and_b32 v235, 16, v226\n"
  "v_mfma_f32_16x16x32_bf16 v[108:111], a[236:239], v[184:187], v[108:111]\n"
  "v_mov_b32 v250, 0x3f80\n"
  "v_lshlrev_b32 v235, v235, v250\n"
  "v_mfma_f32_16x16x32_bf16 v[112:115], a[240:243], v[184:187], v[112:115]\n"
  "v_cndmask_b32 v235, v236, v235, s[66:67]\n"
  "v_and_b32 v250, 32, v226\n"
  "v_mfma_f32_16x16x32_bf16 v[116:119], a[244:247], v[184:187], v[116:119]\n"
  "buffer_load_dwordx4 a[124:127], v226, s[24:27], s61 offen offset:3072 sc1\n"
  "v_cmp_eq_u32 s[68:69], 0, v250\n"
  "v_mfma_f32_16x16x32_bf16 v[120:123], a[248:251], v[184:187], v[120:123]\n"
  "v_mov_b32 v250, 0\n"
  "v_mov_b32 v251, 0\n"
  "v_mfma_f32_16x16x32_bf16 v[124:127], a[252:255], v[184:187], v[124:127]\n"
  "v_mov_b32 v252, 0\n"
  "v_mov_b32 v253, 0\n"
  "v_cndmask_b32 v248, v236, v235, s[68:69]\n"
  "v_cndmask_b32 v249, v235, v236, s[68:69]\n"
  "v_mov_b32 v254, v248\n"
  "v_mov_b32 v255, v249\n"
  "s_cmp_lg_u32 s77, -1\n"
  "s_cbranch_scc1 L_Zp\n"
  "s_cmp_eq_u32 s74, 0\n"
  "s_cbranch_scc1 L_Ze\n"
  "s_cmp_eq_u32 s75, 0\n"
  "s_cbranch_scc1 L_Zc0\n"
  "s_waitcnt vmcnt(17)\n"
  "v_add_f32 v192, v192, v244\n"
  "v_add_f32 v196, v196, v245\n"
  "v_add_f32 v200, v200, v246\n"
  "v_add_f32 v204, v204, v247\n"
  "v_mfma_f32_16x16x32_bf16 v[0:3], v[248:251], a[128:131], v[0:3]\n"
  "v_mfma_f32_16x16x32_bf16 v[4:7], v[252:255], a[128:131], v[4:7]\n"
  "v_mfma_f32_16x16x32_bf16 v[8:11], v[248:251], a[132:135], v[8:11]\n"
  "v_mfma_f32_16x16x32_bf16 v[12:15], v[252:255], a[132:135], v[12:15]\n"
  "v_mfma_f32_16x16x32_bf16 v[16:19], v[248:251], a[136:139], v[16:19]\n"
  "v_mfma_f32_16x16x32_bf16 v[20:23], v[252:255], a[136:139], v[20:23]\n"
  "v_mfma_f32_16x16x32_bf16 v[24:27], v[248:251], a[140:143], v[24:27]\n"
  "v_mfma_f32_16x16x32_bf16 v[28:31], v[252:255], a[140:143], v[28:31]\n"
  "v_mfma_f32_16x16x32_bf16 v[32:35], v[248:251], a[144:147], v[32:35]\n"
  "v_mfma_f32_16x16x32_bf16 v[36:39], v[252:255], a[144:147], v[36:39]\n"
  "v_mfma_f32_16x16x32_bf16 v[40:43], v[248:251], a[148:151], v[40:43]\n"
  "v_mfma_f32_16x16x32_bf16 v[44:47], v[252:255], a[148:151], v[44:47]\n"
  "v_mfma_f32_16x16x32_bf16 v[48:51], v[248:251], a[152:155], v[48:51]\n"
  "v_mfma_f32_16x16x32_bf16 v[52:55], v[252:255], a[152:155], v[52:55]\n"
  "v_mfma_f32_16x16x32_bf16 v[56:59], v[248:251], a[156:159], v[56:59]\n"
  "v_mfma_f32_16x16x32_bf16 v[60:63], v[252:255], a[156:159], v[60:63]\n"
  "v_mfma_f32_16x16x32_bf16 v[64:67], v[248:251], a[160:163], v[64:67]\n"
  "v_mfma_f32_16x16x32_bf16 v[68:71], v[252:255], a[160:163], v[68:71]\n"
  "v_mfma_f32_16x16x32_bf16 v[72:75], v[248:251], a[164:167], v[72:75]\n"
  "v_mfma_f32_16x16x32_bf16 v[76:79], v[252:255], a[164:167], v[76:79]\n"
  "v_mfma_f32_16x16x32_bf16 v[80:83], v[248:251], a[168:171], v[80:83]\n"
  "v_mfma_f32_16x16x32_bf16 v[84:87], v[252:255], a[168:171], v[84:87]\n"
  "v_mfma_f32_16x16x32_bf16 v[88:91], v[248:251], a[172:175], v[88:91]\n"
  "v_mfma_f32_16x16x32_bf16 v[92:95], v[252:255], a[172:175], v[92:95]\n"
  "v_mfma_f32_16x16x32_bf16 v[96:99], v[248:251], a[176:179], v[96:99]\n"
  "v_mfma_f32_16x16x32_bf16 v[100:103], v[252:255], a[176:179], v[100:103]\n"
  "v_mfma_f32_16x16x32_bf16 v[104:107], v[248:251], a[180:183], v[104:107]\n"
  "v_mfma_f32_16x16x32_bf16 v[108:111], v[252:255], a[180:183], v[108:111]\n"
  "v_mfma_f32_16x16x32_bf16 v[112:115], v[248:251], a[184:187], v[112:115]\n"
  "v_mfma_f32_16x16x32_bf16 v[116:119], v[252:255], a[184:187], v[116:119]\n"
  "v_mfma_f32_16x16x32_bf16 v[120:123], v[248:251], a[188:191], v[120:123]\n"
  "v_mfma_f32_16x16x32_bf16 v[124:127], v[252:255], a[188:191], v[124:127]\n"
  "L_Zc0:\n"
  "s_cmp_ge_u32 s76, 2146435072\n"
  "s_cbranch_scc1 L_Zslow\n"
  "L_Zc1:\n"
  "s_waitcnt vmcnt(12)\n"
  "v_add_f32 v192, v192, v240\n"
  "v_add_f32 v196, v196, v241\n"
  "v_add_f32 v200, v200, v242\n"
  "v_add_f32 v204, v204, v243\n"
  "v_mfma_f32_16x16x32_bf16 v[0:3], v[248:251], a[64:67], v[0:3]\n"
  "v_med3_f32 v193, v192, s88, v234\n"
  "v_mfma_f32_16x16x32_bf16 v[4:7], v[252:255], a[64:67], v[4:7]\n"
  "v_rcp_f32 v194, v192\n"
  "v_mfma_f32_16x16x32_bf16 v[8:11], v[248:251], a[68:71], v[8:11]\n"
  "v_cmp_neq_f32 s[62:63], v193, v192\n"
  "v_med3_f32 v197, v196, s88, v234\n"
  "v_mfma_f32_16x16x32_bf16 v[12:15], v[252:255], a[68:71], v[12:15]\n"
  "v_rcp_f32 v198, v196\n"
  "v_mfma_f32_16x16x32_bf16 v[16:19], v[248:251], a[72:75], v[16:19]\n"
  "v_cmp_neq_f32 s[64:65], v197, v196\n"
  "v_mfma_f32_16x16x32_bf16 v[20:23], v[252:255], a[72:75], v[20:23]\n"
  "v_mfma_f32_16x16x32_bf16 v[24:27], v[248:251], a[76:79], v[24:27]\n"
  "v_mfma_f32_16x16x32_bf16 v[28:31], v[252:255], a[76:79], v[28:31]\n"
  "s_waitcnt vmcnt(8)\n"
  "v_mfma_f32_16x16x32_bf16 v[32:35], v[248:251], a[80:83], v[32:35]\n"
  "v_med3_f32 v201, v200, s88, v234\n"
  "v_mfma_f32_16x16x32_bf16 v[36:39], v[252:255], a[80:83], v[36:39]\n"
  "v_rcp_f32 v202, v200\n"
  "v_mfma_f32_16x16x32_bf16 v[40:43], v[248:251], a[84:87], v[40:43]\n"
  "v_cmp_neq_f32 s[66:67], v201, v200\n"
  "v_med3_f32 v205, v204, s88, v234\n"
  "v_mfma_f32_16x16x32_bf16 v[44:47], v[252:255], a[84:87], v[44:47]\n"
  "v_rcp_f32 v206, v204\n"
  "v_mfma_f32_16x16x32_bf16 v[48:51], v[248:251], a[88:91], v[48:51]\n"
  "v_cmp_neq_f32 s[68:69], v205, v204\n"
  "v_mfma_f32_16x16x32_bf16 v[52:55], v[252:255], a[88:91], v[52:55]\n"
  "v_mfma_f32_16x16x32_bf16 v[56:59], v[248:251], a[92:95], v[56:59]\n"
  "v_mfma_f32_16x16x32_bf16 v[60:63], v[252:255], a[92:95], v[60:63]\n"
  "s_waitcnt vmcnt(4)\n"
  "v_mfma_f32_16x16x32_bf16 v[64:67], v[248:251], a[96:99], v[64:67]\n"
  "s_cmp_ge_u32 s35, 2146435072\n"
  "s_cselect_b64 s[64:65], 0, s[64:65]\n"
  "v_mfma_f32_16x16x32_bf16 v[68:71], v[252:255], a[96:99], v[68:71]\n"
  "s_or_b64 s[62:63], s[62:63], s[64:65]\n"
  "s_cmp_ge_u32 s36, 2146435072\n"
  "v_mfma_f32_16x16x32_bf16 v[72:75], v[248:251], a[100:103], v[72:75]\n"
  "s_cselect_b64 s[66:67], 0, s[66:67]\n"
  "s_or_b64 s[62:63], s[62:63], s[66:67]\n"
  "v_mfma_f32_16x16x32_bf16 v[76:79], v[252:255], a[100:103], v[76:79]\n"
  "s_cmp_ge_u32 s37, 2146435072\n"
  "s_cselect_b64 s[68:69], 0, s[68:69]\n"
  "v_mfma_f32_16x16x32_bf16 v[80:83], v[248:251], a[104:107], v[80:83]\n"
  "s_or_b64 s[62:63], s[62:63], s[68:69]\n"
  "s_cselect_b32 s58, 1, 0\n"
  "v_mfma_f32_16x16x32_bf16 v[84:87], v[252:255], a[104:107], v[84:87]\n"
  "s_lshl_b32 s58, s58, s80\n"
  "s_or_b32 s54, s54, s58\n"
  "v_mfma_f32_16x16x32_bf16 v[88:91], v[248:251], a[108:111], v[88:91]\n"
  "v_mfma_f32_16x16x32_bf16 v[92:95], v[252:255], a[108:111], v[92:95]\n"
  "s_waitcnt vmcnt(0)\n"
  "v_mfma_f32_16x16x32_bf16 v[96:99], v[248:251], a[112:115], v[96:99]\n"
  "v_mfma_f32_16x16x32_bf16 v[100:103], v[252:255], a[112:115], v[100:103]\n"
  "v_mfma_f32_16x16x32_bf16 v[104:107], v[248:251], a[116:119], v[104:107]\n"
  "v_mfma_f32_16x16x32_bf16 v[108:111], v[252:255], a[116:119], v[108:111]\n"
  "v_mfma_f32_16x16x32_bf16 v[112:115], v[248:251], a[120:123], v[112:115]\n"
  "v_mfma_f32_16x16x32_bf16 v[116:119], v[252:255], a[120:123], v[116:119]\n"
  "v_mfma_f32_16x16x32_bf16 v[120:123], v[248:251], a[124:127], v[120:123]\n"
  "v_mfma_f32_16x16x32_bf16 v[124:127], v[252:255], a[124:127], v[124:127]\n"
  "s_branch L_Ze2\n"
  "L_Ze:\n"
  "v_med3_f32 v193, v192, s88, v234\n"
  "v_rcp_f32 v194, v192\n"
  "v_cmp_neq_f32 s[62:63], v193, v192\n"
  "v_med3_f32 v197, v196, s88, v234\n"
  "v_rcp_f32 v198, v196\n"
  "v_cmp_neq_f32 s[64:65], v197, v196\n"
  "v_med3_f32 v201, v200, s88, v234\n"
  "v_rcp_f32 v202, v200\n"
  "v_cmp_neq_f32 s[66:67], v201, v200\n"
  "v_med3_f32 v205, v204, s88, v234\n"
  "v_rcp_f32 v206, v204\n"
  "v_cmp_neq_f32 s[68:69], v205, v204\n"
  "s_cmp_ge_u32 s35, 2146435072\n"
  "s_cselect_b64 s[64:65], 0, s[64:65]\n"
  "s_or_b64 s[62:63], s[62:63], s[64:65]\n"
  "s_cmp_ge_u32 s36, 2146435072\n"
  "s_cselect_b64 s[66:67], 0, s[66:67]\n"
  "s_or_b64 s[62:63], s[62:63], s[66:67]\n"
  "s_cmp_ge_u32 s37, 2146435072\n"
  "s_cselect_b64 s[68:69], 0, s[68:69]\n"
  "s_or_b64 s[62:63], s[62:63], s[68:69]\n"
  "s_cselect_b32 s58, 1, 0\n"
  "s_lshl_b32 s58, s58, s80\n"
  "s_or_b32 s54, s54, s58\n"
  "L_Ze2:\n"
  "v_pk_mul_f32 v[0:1], v[0:1], v[194:195] op_sel_hi:[1,0]\n"
  "v_pk_mul_f32 v[2:3], v[2:3], v[194:195] op_sel_hi:[1,0]\n"
  "v_pk_mul_f32 v[4:5], v[4:5], v[194:195] op_sel_hi:[1,0]\n"
  "v_pk_mul_f32 v[6:7], v[6:7], v[194:195] op_sel_hi:[1,0]\n"
  "v_pk_mul_f32 v[8:9], v[8:9], v[194:195] op_sel_hi:[1,0]\n"
  "v_pk_mul_f32 v[10:11], v[10:11], v[194:195] op_sel_hi:[1,0]\n"
  "v_pk_mul_f32 v[12:13], v[12:13], v[194:195] op_sel_hi:[1,0]\n"
  "v_pk_mul_f32 v[14:15], v[14:15], v[194:195] op_sel_hi:[1,0]\n"
  "v_pk_mul_f32 v[16:17], v[16:17], v[194:195] op_sel_hi:[1,0]\n"
  "v_pk_mul_f32 v[18:19], v[18:19], v[194:195] op_sel_hi:[1,0]\n"
  "v_pk_mul_f32 v[20:21], v[20:21], v[194:195] op_sel_hi:[1,0]\n"
  "v_pk_mul_f32 v[22:23], v[22:23], v[194:195] op_sel_hi:[1,0]\n"
  "v_pk_mul_f32 v[24:25], v[24:25], v[194:195] op_sel_hi:[1,0]\n"
  "v_pk_mul_f32 v[26:27], v[26:27], v[194:195] op_sel_hi:[1,0]\n"
  "v_pk_mul_f32 v[28:29], v[28:29], v[194:195] op_sel_hi:[1,0]\n"
  "v_pk_mul_f32 v[30:31], v[30:31], v[194:195] op_sel_hi:[1,0]\n"
  "v_cvt_pk_bf16_f32 v0, v0, v1\n"
  "v_cvt_pk_bf16_f32 v1, v2, v3\n"
  "v_cvt_pk_bf16_f32 v4, v4, v5\n"
  "v_cvt_pk_bf16_f32 v5, v6, v7\n"
  "v_cvt_pk_bf16_f32 v8, v8, v9\n"
  "v_cvt_pk_bf16_f32 v9, v10, v11\n"
  "v_cvt_pk_bf16_f32 v12, v12, v13\n"
  "v_cvt_pk_bf16_f32 v13, v14, v15\n"
  "v_cvt_pk_bf16_f32 v16, v16, v17\n"
  "v_cvt_pk_bf16_f32 v17, v18, v19\n"
  "v_cvt_pk_bf16_f32 v20, v20, v21\n"
  "v_cvt_pk_bf16_f32 v21, v22, v23\n"
  "v_cvt_pk_bf16_f32 v24, v24, v25\n"
  "v_cvt_pk_bf16_f32 v25, v26, v27\n"
  "v_cvt_pk_bf16_f32 v28, v28, v29\n"
  "v_cvt_pk_bf16_f32 v29, v30, v31\n"
  "ds_write_b64 v128, v[0:1] offset:0\n"
  "ds_write_b64 v129, v[4:5] offset:0\n"
  "ds_write_b64 v130, v[8:9] offset:0\n"
  "ds_write_b64 v131, v[12:13] offset:0\n"
  "ds_write_b64 v128, v[16:17] offset:128\n"
  "ds_write_b64 v129, v[20:21] offset:128\n"
  "ds_write_b64 v130, v[24:25] offset:128\n"
  "ds_write_b64 v131, v[28:29] offset:128\n"
  "s_cmp_eq_u32 s78, 1\n"
  "s_cbranch_scc1 L_Ze_x1\n"
  "v_pk_mul_f32 v[64:65], v[64:65], v[202:203] op_sel_hi:[1,0]\n"
  "v_pk_mul_f32 v[66:67], v[66:67], v[202:203] op_sel_hi:[1,0]\n"
  "v_pk_mul_f32 v[68:69], v[68:69], v[202:203] op_sel_hi:[1,0]\n"
  "v_pk_mul_f32 v[70:71], v[70:71], v[202:203] op_sel_hi:[1,0]\n"
  "v_pk_mul_f32 v[72:73], v[72:73], v[202:203] op_sel_hi:[1,0]\n"
  "v_pk_mul_f32 v[74:75], v[74:75], v[202:203] op_sel_hi:[1,0]\n"
  "v_pk_mul_f32 v[76:77], v[76:77], v[202:203] op_sel_hi:[1,0]\n"
  "v_pk_mul_f32 v[78:79], v[78:79], v[202:203] op_sel_hi:[1,0]\n"
  "v_pk_mul_f32 v[80:81], v[80:81], v[202:203] op_sel_hi:[1,0]\n"
  "v_pk_mul_f32 v[82:83], v[82:83], v[202:203] op_sel_hi:[1,0]\n"
  "v_pk_mul_f32 v[84:85], v[84:85], v[202:203] op_sel_hi:[1,0]\n"
  "v_pk_mul_f32 v[86:87], v[86:87], v[202:203] op_sel_hi:[1,0]\n"
  "v_pk_mul_f32 v[88:89], v[88:89], v[202:203] op_sel_hi:[1,0]\n"
  "v_pk_mul_f32 v[90:91], v[90:91], v[202:203] op_sel_hi:[1,0]\n"
  "v_pk_mul_f32 v[92:93], v[92:93], v[202:203] op_sel_hi:[1,0]\n"
  "v_pk_mul_f32 v[94:95], v[94:95], v[202:203] op_sel_hi:[1,0]\n"
  "s_waitcnt lgkmcnt(0)\n"
  "ds_read_b128 v[0:3], v132 offset:0\n"
  "ds_read_b128 v[4:7], v133 offset:1024\n"
  "ds_read_b128 v[8:11], v132 offset:2048\n"
  "ds_read_b128 v[12:15], v133 offset:3072\n"
  "v_cvt_pk_bf16_f32 v64, v64, v65\n"
  "v_cvt_pk_bf16_f32 v65, v66, v67\n"
  "v_cvt_pk_bf16_f32 v68, v68, v69\n"
  "v_cvt_pk_bf16_f32 v69, v70, v71\n"
  "v_cvt_pk_bf16_f32 v72, v72, v73\n"
  "v_cvt_pk_bf16_f32 v73, v74, v75\n"
  "v_cvt_pk_bf16_f32 v76, v76, v77\n"
  "v_cvt_pk_bf16_f32 v77, v78, v79\n"
  "v_cvt_pk_bf16_f32 v80, v80, v81\n"
  "v_cvt_pk_bf16_f32 v81, v82, v83\n"
  "v_cvt_pk_bf16_f32 v84, v84, v85\n"
  "v_cvt_pk_bf16_f32 v85, v86, v87\n"
  "v_cvt_pk_bf16_f32 v88, v88, v89\n"
  "v_cvt_pk_bf16_f32 v89, v90, v91\n"
  "v_cvt_pk_bf16_f32 v92, v92, v93\n"
  "v_cvt_pk_bf16_f32 v93, v94, v95\n"
  "ds_write_b64 v128, v[64:65] offset:8192\n"
  "ds_write_b64 v129, v[68:69] offset:8192\n"
  "ds_write_b64 v130, v[72:73] offset:8192\n"
  "ds_write_b64 v131, v[76:77] offset:8192\n"
  "ds_write_b64 v128, v[80:81] offset:8320\n"
  "ds_write_b64 v129, v[84:85] offset:8320\n"
  "ds_write_b64 v130, v[88:89] offset:8320\n"
  "ds_write_b64 v131, v[92:93] offset:8320\n"
  "s_waitcnt lgkmcnt(8)\n"
  "buffer_store_dwordx4 v[0:3], v229, s[20:23], s34 offen sc1 nt\n"
  "s_add_u32 s34, s34, 0x4000\n"
  "buffer_store_dwordx4 v[4:7], v229, s[20:23], s34 offen sc1 nt\n"
  "s_add_u32 s34, s34, 0x4000\n"
  "buffer_store_dwordx4 v[8:11], v229, s[20:23], s34 offen sc1 nt\n"
  "s_add_u32 s34, s34, 0x4000\n"
  "buffer_store_dwordx4 v[12:15], v229, s[20:23], s34 offen sc1 nt\n"
  "s_cmp_eq_u32 s78, 2\n"
  "s_cbranch_scc1 L_Ze_x2\n"
  "v_pk_mul_f32 v[32:33], v[32:33], v[198:199] op_sel_hi:[1,0]\n"
  "v_pk_mul_f32 v[34:35], v[34:35], v[198:199] op_sel_hi:[1,0]\n"
  "v_pk_mul_f32 v[36:37], v[36:37], v[198:199] op_sel_hi:[1,0]\n"
  "v_pk_mul_f32 v[38:39], v[38:39], v[198:199] op_sel_hi:[1,0]\n"
  "v_pk_mul_f32 v[40:41], v[40:41], v[198:199] op_sel_hi:[1,0]\n"
  "v_pk_mul_f32 v[42:43], v[42:43], v[198:199] op_sel_hi:[1,0]\n"
  "v_pk_mul_f32 v[44:45], v[44:45], v[198:199] op_sel_hi:[1,0]\n"
  "v_pk_mul_f32 v[46:47], v[46:47], v[198:199] op_sel_hi:[1,0]\n"
  "v_pk_mul_f32 v[48:49], v[48:49], v[198:199] op_sel_hi:[1,0]\n"
  "v_pk_mul_f32 v[50:51], v[50:51], v[198:199] op_sel_hi:[1,0]\n"
  "v_pk_mul_f32 v[52:53], v[52:53], v[198:199] op_sel_hi:[1,0]\n"
  "v_pk_mul_f32 v[54:55], v[54:55], v[198:199] op_sel_hi:[1,0]\n"
  "v_pk_mul_f32 v[56:57], v[56:57], v[198:199] op_sel_hi:[1,0]\n"
  "v_pk_mul_f32 v[58:59], v[58:59], v[198:199] op_sel_hi:[1,0]\n"
  "v_pk_mul_f32 v[60:61], v[60:61], v[198:199] op_sel_hi:[1,0]\n"
  "v_pk_mul_f32 v[62:63], v[62:63], v[198:199] op_sel_hi:[1,0]\n"
  "s_waitcnt lgkmcnt(0)\n"
  "ds_read_b128 v[64:67], v132 offset:8192\n"
  "ds_read_b128 v[68:71], v133 offset:9216\n"
  "ds_read_b128 v[72:75], v132 offset:10240\n"
  "ds_read_b128 v[76:79], v133 offset:11264\n"
  "v_cvt_pk_bf16_f32 v32, v32, v33\n"
  "v_cvt_pk_bf16_f32 v33, v34, v35\n"
  "v_cvt_pk_bf16_f32 v36, v36, v37\n"
  "v_cvt_pk_bf16_f32 v37, v38, v39\n"
  "v_cvt_pk_bf16_f32 v40, v40, v41\n"
  "v_cvt_pk_bf16_f32 v41, v42, v43\n"
  "v_cvt_pk_bf16_f32 v44, v44, v45\n"
  "v_cvt_pk_bf16_f32 v45, v46, v47\n"
  "v_cvt_pk_bf16_f32 v48, v48, v49\n"
  "v_cvt_pk_bf16_f32 v49, v50, v51\n"
  "v_cvt_pk_bf16_f32 v52, v52, v53\n"
  "v_cvt_pk_bf16_f32 v53, v54, v55\n"
  "v_cvt_pk_bf16_f32 v56, v56, v57\n"
  "v_cvt_pk_bf16_f32 v57, v58, v59\n"
  "v_cvt_pk_bf16_f32 v60, v60, v61\n"
  "v_cvt_pk_bf16_f32 v61, v62, v63\n"
  "ds_write_b64 v128, v[32:33] offset:4096\n"
  "ds_write_b64 v129, v[36:37] offset:4096\n"
  "ds_write_b64 v130, v[40:41] offset:4096\n"
  "ds_write_b64 v131, v[44:45] offset:4096\n"
  "ds_write_b64 v128, v[48:49] offset:4224\n"
  "ds_write_b64 v129, v[52:53] offset:4224\n"
  "ds_write_b64 v130, v[56:57] offset:4224\n"
  "ds_write_b64 v131, v[60:61] offset:4224\n"
  "s_waitcnt lgkmcnt(8)\n"
  "buffer_store_dwordx4 v[64:67], v229, s[20:23], s36 offen sc1 nt\n"
  "s_add_u32 s36, s36, 0x4000\n"
  "buffer_store_dwordx4 v[68:71], v229, s[20:23], s36 offen sc1 nt\n"
  "s_add_u32 s36, s36, 0x4000\n"
  "buffer_store_dwordx4 v[72:75], v229, s[20:23], s36 offen sc1 nt\n"
  "s_add_u32 s36, s36, 0x4000\n"
  "buffer_store_dwordx4 v[76:79], v229, s[20:23], s36 offen sc1 nt\n"
  "s_cmp_eq_u32 s78, 3\n"
  "s_cbranch_scc1 L_Ze_x3\n"
  "v_pk_mul_f32 v[96:97], v[96:97], v[206:207] op_sel_hi:[1,0]\n"
  "v_pk_mul_f32 v[98:99], v[98:99], v[206:207] op_sel_hi:[1,0]\n"
  "v_pk_mul_f32 v[100:101], v[100:101], v[206:207] op_sel_hi:[1,0]\n"
  "v_pk_mul_f32 v[102:103], v[102:103], v[206:207] op_sel_hi:[1,0]\n"
  "v_pk_mul_f32 v[104:105], v[104:105], v[206:207] op_sel_hi:[1,0]\n"
  "v_pk_mul_f32 v[106:107], v[106:107], v[206:207] op_sel_hi:[1,0]\n"
  "v_pk_mul_f32 v[108:109], v[108:109], v[206:207] op_sel_hi:[1,0]\n"
  "v_pk_mul_f32 v[110:111], v[110:111], v[206:207] op_sel_hi:[1,0]\n"
  "v_pk_mul_f32 v[112:113], v[112:113], v[206:207] op_sel_hi:[1,0]\n"
  "v_pk_mul_f32 v[114:115], v[114:115], v[206:207] op_sel_hi:[1,0]\n"
  "v_pk_mul_f32 v[116:117], v[116:117], v[206:207] op_sel_hi:[1,0]\n"
  "v_pk_mul_f32 v[118:119], v[118:119], v[206:207] op_sel_hi:[1,0]\n"
  "v_pk_mul_f32 v[120:121], v[120:121], v[206:207] op_sel_hi:[1,0]\n"
  "v_pk_mul_f32 v[122:123], v[122:123], v[206:207] op_sel_hi:[1,0]\n"
  "v_pk_mul_f32 v[124:125], v[124:125], v[206:207] op_sel_hi:[1,0]\n"
  "v_pk_mul_f32 v[126:127], v[126:127], v[206:207] op_sel_hi:[1,0]\n"
  "s_waitcnt lgkmcnt(0)\n"
  "ds_read_b128 v[32:35], v132 offset:4096\n"
  "ds_read_b128 v[36:39], v133 offset:5120\n"
  "ds_read_b128 v[40:43], v132 offset:6144\n"
  "ds_read_b128 v[44:47], v133 offset:7168\n"
  "v_cvt_pk_bf16_f32 v96, v96, v97\n"
  "v_cvt_pk_bf16_f32 v97, v98, v99\n"
  "v_cvt_pk_bf16_f32 v100, v100, v101\n"
  "v_cvt_pk_bf16_f32 v101, v102, v103\n"
  "v_cvt_pk_bf16_f32 v104, v104, v105\n"
  "v_cvt_pk_bf16_f32 v105, v106, v107\n"
  "v_cvt_pk_bf16_f32 v108, v108, v109\n"
  "v_cvt_pk_bf16_f32 v109, v110, v111\n"
  "v_cvt_pk_bf16_f32 v112, v112, v113\n"
  "v_cvt_pk_bf16_f32 v113, v114, v115\n"
  "v_cvt_pk_bf16_f32 v116, v116, v117\n"
  "v_cvt_pk_bf16_f32 v117, v118, v119\n"
  "v_cvt_pk_bf16_f32 v120, v120, v121\n"
  "v_cvt_pk_bf16_f32 v121, v122, v123\n"
  "v_cvt_pk_bf16_f32 v124, v124, v125\n"
  "v_cvt_pk_bf16_f32 v125, v126, v127\n"
  "ds_write_b64 v128, v[96:97] offset:12288\n"
  "ds_write_b64 v129, v[100:101] offset:12288\n"
  "ds_write_b64 v130, v[104:105] offset:12288\n"
  "ds_write_b64 v131, v[108:109] offset:12288\n"
  "ds_write_b64 v128, v[112:113] offset:12416\n"
  "ds_write_b64 v129, v[116:117] offset:12416\n"
  "ds_write_b64 v130, v[120:121] offset:12416\n"
  "ds_write_b64 v131, v[124:125] offset:12416\n"
  "s_waitcnt lgkmcnt(8)\n"
  "buffer_store_dwordx4 v[32:35], v229, s[20:23], s35 offen sc1 nt\n"
  "s_add_u32 s35, s35, 0x4000\n"
  "buffer_store_dwordx4 v[36:39], v229, s[20:23], s35 offen sc1 nt\n"
  "s_add_u32 s35, s35, 0x4000\n"
  "buffer_store_dwordx4 v[40:43], v229, s[20:23], s35 offen sc1 nt\n"
  "s_add_u32 s35, s35, 0x4000\n"
  "buffer_store_dwordx4 v[44:47], v229, s[20:23], s35 offen sc1 nt\n"
  "s_waitcnt lgkmcnt(0)\n"
  "ds_read_b128 v[96:99], v132 offset:12288\n"
  "ds_read_b128 v[100:103], v133 offset:13312\n"
  "ds_read_b128 v[104:107], v132 offset:14336\n"
  "ds_read_b128 v[108:111], v133 offset:15360\n"
  "s_waitcnt lgkmcnt(0)\n"
  "buffer_store_dwordx4 v[96:99], v229, s[20:23], s37 offen sc1 nt\n"
  "s_add_u32 s37, s37, 0x4000\n"
  "buffer_store_dwordx4 v[100:103], v229, s[20:23], s37 offen sc1 nt\n"
  "s_add_u32 s37, s37, 0x4000\n"
  "buffer_store_dwordx4 v[104:107], v229, s[20:23], s37 offen sc1 nt\n"
  "s_add_u32 s37, s37, 0x4000\n"
  "buffer_store_dwordx4 v[108:111], v229, s[20:23], s37 offen sc1 nt\n"
  "s_branch L_exit\n"
  "L_Ze_x1:\n"
  "s_waitcnt lgkmcnt(0)\n"
  "ds_read_b128 v[0:3], v132 offset:0\n"
  "ds_read_b128 v[4:7], v133 offset:1024\n"
  "ds_read_b128 v[8:11], v132 offset:2048\n"
  "ds_read_b128 v[12:15], v133 offset:3072\n"
  "s_waitcnt lgkmcnt(0)\n"
  "buffer_store_dwordx4 v[0:3], v229, s[20:23], s34 offen sc1 nt\n"
  "s_add_u32 s34, s34, 0x4000\n"
  "buffer_store_dwordx4 v[4:7], v229, s[20:23], s34 offen sc1 nt\n"
  "s_add_u32 s34, s34, 0x4000\n"
  "buffer_store_dwordx4 v[8:11], v229, s[20:23], s34 offen sc1 nt\n"
  "s_add_u32 s34, s34, 0x4000\n"
  "buffer_store_dwordx4 v[12:15], v229, s[20:23], s34 offen sc1 nt\n"
  "s_branch L_exit\n"
  "L_Ze_x2:\n"
  "s_waitcnt lgkmcnt(0)\n"
  "ds_read_b128 v[64:67], v132 offset:8192\n"
  "ds_read_b128 v[68:71], v133 offset:9216\n"
  "ds_read_b128 v[72:75], v132 offset:10240\n"
  "ds_read_b128 v[76:79], v133 offset:11264\n"
  "s_waitcnt lgkmcnt(0)\n"
  "buffer_store_dwordx4 v[64:67], v229, s[20:23], s36 offen sc1 nt\n"
  "s_add_u32 s36, s36, 0x4000\n"
  "buffer_store_dwordx4 v[68:71], v229, s[20:23], s36 offen sc1 nt\n"
  "s_add_u32 s36, s36, 0x4000\n"
  "buffer_store_dwordx4 v[72:75], v229, s[20:23], s36 offen sc1 nt\n"
  "s_add_u32 s36, s36, 0x4000\n"
  "buffer_store_dwordx4 v[76:79], v229, s[20:23], s36 offen sc1 nt\n"
  "s_branch L_exit\n"
  "L_Ze_x3:\n"
  "s_waitcnt lgkmcnt(0)\n"
  "ds_read_b128 v[32:35], v132 offset:4096\n"
  "ds_read_b128 v[36:39], v133 offset:5120\n"
  "ds_read_b128 v[40:43], v132 offset:6144\n"
  "ds_read_b128 v[44:47], v133 offset:7168\n"
  "s_waitcnt lgkmcnt(0)\n"
  "buffer_store_dwordx4 v[32:35], v229, s[20:23], s35 offen sc1 nt\n"
  "s_add_u32 s35, s35, 0x4000\n"
  "buffer_store_dwordx4 v[36:39], v229, s[20:23], s35 offen sc1 nt\n"
  "s_add_u32 s35, s35, 0x4000\n"
  "buffer_store_dwordx4 v[40:43], v229, s[20:23], s35 offen sc1 nt\n"
  "s_add_u32 s35, s35, 0x4000\n"
  "buffer_store_dwordx4 v[44:47], v229, s[20:23], s35 offen sc1 nt\n"
  "s_branch L_exit\n"
  "L_Zslow:\n"
  "s_mov_b32 s61, 20000\n"
  "L_Zspin:\n"
  "buffer_load_dwordx2 v[230:231], v227, s[24:27], s50 offen sc1\n"
  "s_waitcnt vmcnt(0)\n"
  "v_readfirstlane_b32 s62, v230\n"
  "v_readfirstlane_b32 s63, v231\n"
  "s_nop 0\n"
  "s_cmp_eq_u64 s[62:63], s[56:57]\n"
  "s_cbranch_scc1 L_Zgot\n"
  "s_sleep 1\n"
  "s_sub_u32 s61, s61, 1\n"
  "s_cbranch_scc0 L_Zspin\n"
  "s_bitset1_b32 s54, s80\n"
  "s_branch L_Ze\n"
  "L_Zgot:\n"
  "s_add_u32 s58, s73, s81\n"
  "buffer_load_dwordx4 a[64:67], v226, s[24:27], s58 offen offset:0 sc1\n"
  "buffer_load_dwordx4 a[68:71], v226, s[24:27], s58 offen offset:1024 sc1\n"
  "buffer_load_dwordx4 a[72:75], v226, s[24:27], s58 offen offset:2048 sc1\n"
  "buffer_load_dwordx4 a[76:79], v226, s[24:27], s58 offen offset:3072 sc1\n"
  "s_add_u32 s58, s58, 4096\n"
  "buffer_load_dwordx4 a[80:83], v226, s[24:27], s58 offen offset:0 sc1\n"
  "buffer_load_dwordx4 a[84:87], v226, s[24:27], s58 offen offset:1024 sc1\n"
  "buffer_load_dwordx4 a[88:91], v226, s[24:27], s58 offen offset:2048 sc1\n"
  "buffer_load_dwordx4 a[92:95], v226, s[24:27], s58 offen offset:3072 sc1\n"
  "s_add_u32 s58, s58, 4096\n"
  "buffer_load_dwordx4 a[96:99], v226, s[24:27], s58 offen offset:0 sc1\n"
  "buffer_load_dwordx4 a[100:103], v226, s[24:27], s58 offen offset:1024 sc1\n"
  "buffer_load_dwordx4 a[104:107], v226, s[24:27], s58 offen offset:2048 sc1\n"
  "buffer_load_dwordx4 a[108:111], v226, s[24:27], s58 offen offset:3072 sc1\n"
  "s_add_u32 s58, s58, 4096\n"
  "buffer_load_dwordx4 a[112:115], v226, s[24:27], s58 offen offset:0 sc1\n"
  "buffer_load_dwordx4 a[116:119], v226, s[24:27], s58 offen offset:1024 sc1\n"
  "buffer_load_dwordx4 a[120:123], v226, s[24:27], s58 offen offset:2048 sc1\n"
  "buffer_load_dwordx4 a[124:127], v226, s[24:27], s58 offen offset:3072 sc1\n"
  "s_lshl_b32 s59, s53, 10\n"
  "s_add_u32 s58, s73, s59\n"
  "s_add_u32 s58, s58, 65536\n"
  "buffer_load_dwordx4 v[240:243], v226, s[24:27], s58 offen sc1\n"
  "s_add_u32 s73, s73, 69888\n"
  "s_add_u32 s75, s75, 1\n"
  "s_add_u32 s50, s50, 69888\n"
  "s_cmp_lt_u32 s75, s74\n"
  "s_cselect_b32 s50, s50, 2146435072\n"
  "v_mov_b32 v230, 0\n"
  "v_mov_b32 v231, 0\n"
  "s_waitcnt vmcnt(0)\n"
  "s_cmp_ge_u32 s75, s74\n"
  "s_cbranch_scc1 L_Zc1\n"
  "v_add_f32 v192, v192, v240\n"
  "v_add_f32 v196, v196, v241\n"
  "v_add_f32 v200, v200, v242\n"
  "v_add_f32 v204, v204, v243\n"
  "v_mfma_f32_16x16x32_bf16 v[0:3], v[248:251], a[64:67], v[0:3]\n"
  "v_mfma_f32_16x16x32_bf16 v[4:7], v[252:255], a[64:67], v[4:7]\n"
  "v_mfma_f32_16x16x32_bf16 v[8:11], v[248:251], a[68:71], v[8:11]\n"
  "v_mfma_f32_16x16x32_bf16 v[12:15], v[252:255], a[68:71], v[12:15]\n"
  "v_mfma_f32_16x16x32_bf16 v[16:19], v[248:251], a[72:75], v[16:19]\n"
  "v_mfma_f32_16x16x32_bf16 v[20:23], v[252:255], a[72:75], v[20:23]\n"
  "v_mfma_f32_16x16x32_bf16 v[24:27], v[248:251], a[76:79], v[24:27]\n"
  "v_mfma_f32_16x16x32_bf16 v[28:31], v[252:255], a[76:79], v[28:31]\n"
  "v_mfma_f32_16x16x32_bf16 v[32:35], v[248:251], a[80:83], v[32:35]\n"
  "v_mfma_f32_16x16x32_bf16 v[36:39], v[252:255], a[80:83], v[36:39]\n"
  "v_mfma_f32_16x16x32_bf16 v[40:43], v[248:251], a[84:87], v[40:43]\n"
  "v_mfma_f32_16x16x32_bf16 v[44:47], v[252:255], a[84:87], v[44:47]\n"
  "v_mfma_f32_16x16x32_bf16 v[48:51], v[248:251], a[88:91], v[48:51]\n"
  "v_mfma_f32_16x16x32_bf16 v[52:55], v[252:255], a[88:91], v[52:55]\n"
  "v_mfma_f32_16x16x32_bf16 v[56:59], v[248:251], a[92:95], v[56:59]\n"
  "v_mfma_f32_16x16x32_bf16 v[60:63], v[252:255], a[92:95], v[60:63]\n"
  "v_mfma_f32_16x16x32_bf16 v[64:67], v[248:251], a[96:99], v[64:67]\n"
  "v_mfma_f32_16x16x32_bf16 v[68:71], v[252:255], a[96:99], v[68:71]\n"
  "v_mfma_f32_16x16x32_bf16 v[72:75], v[248:251], a[100:103], v[72:75]\n"
  "v_mfma_f32_16x16x32_bf16 v[76:79], v[252:255], a[100:103], v[76:79]\n"
  "v_mfma_f32_16x16x32_bf16 v[80:83], v[248:251], a[104:107], v[80:83]\n"
  "v_mfma_f32_16x16x32_bf16 v[84:87], v[252:255], a[104:107], v[84:87]\n"
  "v_mfma_f32_16x16x32_bf16 v[88:91], v[248:251], a[108:111], v[88:91]\n"
  "v_mfma_f32_16x16x32_bf16 v[92:95], v[252:255], a[108:111], v[92:95]\n"
  "v_mfma_f32_16x16x32_bf16 v[96:99], v[248:251], a[112:115], v[96:99]\n"
  "v_mfma_f32_16x16x32_bf16 v[100:103], v[252:255], a[112:115], v[100:103]\n"
  "v_mfma_f32_16x16x32_bf16 v[104:107], v[248:251], a[116:119], v[104:107]\n"
  "v_mfma_f32_16x16x32_bf16 v[108:111], v[252:255], a[116:119], v[108:111]\n"
  "v_mfma_f32_16x16x32_bf16 v[112:115], v[248:251], a[120:123], v[112:115]\n"
  "v_mfma_f32_16x16x32_bf16 v[116:119], v[252:255], a[120:123], v[116:119]\n"
  "v_mfma_f32_16x16x32_bf16 v[120:123], v[248:251], a[124:127], v[120:123]\n"
  "v_mfma_f32_16x16x32_bf16 v[124:127], v[252:255], a[124:127], v[124:127]\n"
  "s_branch L_Zslow\n"
  "L_Zp:\n"
  "v_mov_b32 v193, v196\n"
  "v_mov_b32 v194, v200\n"
  "v_mov_b32 v195, v204\n"
  "s_lshl_b32 s88, s53, 10\n"
  "s_add_u32 s88, s77, s88\n"
  "s_add_u32 s88, s88, 65536\n"
  "buffer_store_dwordx4 v[192:195], v226, s[24:27], s88 offen sc1\n"
  "v_cvt_pk_bf16_f32 v0, v0, v1\n"
  "v_cvt_pk_bf16_f32 v1, v2, v3\n"
  "v_cvt_pk_bf16_f32 v2, v4, v5\n"
  "v_cvt_pk_bf16_f32 v3, v6, v7\n"
  "v_cvt_pk_bf16_f32 v8, v8, v9\n"
  "v_cvt_pk_bf16_f32 v9, v10, v11\n"
  "v_cvt_pk_bf16_f32 v10, v12, v13\n"
  "v_cvt_pk_bf16_f32 v11, v14, v15\n"
  "v_cvt_pk_bf16_f32 v16, v16, v17\n"
  "v_cvt_pk_bf16_f32 v17, v18, v19\n"
  "v_cvt_pk_bf16_f32 v18, v20, v21\n"
  "v_cvt_pk_bf16_f32 v19, v22, v23\n"
  "v_cvt_pk_bf16_f32 v24, v24, v25\n"
  "v_cvt_pk_bf16_f32 v25, v26, v27\n"
  "v_cvt_pk_bf16_f32 v26, v28, v29\n"
  "v_cvt_pk_bf16_f32 v27, v30, v31\n"
  "s_add_u32 s88, s77, s81\n"
  "buffer_store_dwordx4 v[0:3], v226, s[24:27], s88 offen offset:0 sc1\n"
  "buffer_store_dwordx4 v[8:11], v226, s[24:27], s88 offen offset:1024 sc1\n"
  "buffer_store_dwordx4 v[16:19], v226, s[24:27], s88 offen offset:2048 sc1\n"
  "buffer_store_dwordx4 v[24:27], v226, s[24:27], s88 offen offset:3072 sc1\n"
  "v_cvt_pk_bf16_f32 v32, v32, v33\n"
  "v_cvt_pk_bf16_f32 v33, v34, v35\n"
  "v_cvt_pk_bf16_f32 v34, v36, v37\n"
  "v_cvt_pk_bf16_f32 v35, v38, v39\n"
  "v_cvt_pk_bf16_f32 v40, v40, v41\n"
  "v_cvt_pk_bf16_f32 v41, v42, v43\n"
  "v_cvt_pk_bf16_f32 v42, v44, v45\n"
  "v_cvt_pk_bf16_f32 v43, v46, v47\n"
  "v_cvt_pk_bf16_f32 v48, v48, v49\n"
  "v_cvt_pk_bf16_f32 v49, v50, v51\n"
  "v_cvt_pk_bf16_f32 v50, v52, v53\n"
  "v_cvt_pk_bf16_f32 v51, v54, v55\n"
  "v_cvt_pk_bf16_f32 v56, v56, v57\n"
  "v_cvt_pk_bf16_f32 v57, v58, v59\n"
  "v_cvt_pk_bf16_f32 v58, v60, v61\n"
  "v_cvt_pk_bf16_f32 v59, v62, v63\n"
  "s_add_u32 s88, s88, 4096\n"
  "buffer_store_dwordx4 v[32:35], v226, s[24:27], s88 offen offset:0 sc1\n"
  "buffer_store_dwordx4 v[40:43], v226, s[24:27], s88 offen offset:1024 sc1\n"
  "buffer_store_dwordx4 v[48:51], v226, s[24:27], s88 offen offset:2048 sc1\n"
  "buffer_store_dwordx4 v[56:59], v226, s[24:27], s88 offen offset:3072 sc1\n"
  "v_cvt_pk_bf16_f32 v64, v64, v65\n"
  "v_cvt_pk_bf16_f32 v65, v66, v67\n"
  "v_cvt_pk_bf16_f32 v66, v68, v69\n"
  "v_cvt_pk_bf16_f32 v67, v70, v71\n"
  "v_cvt_pk_bf16_f32 v72, v72, v73\n"
  "v_cvt_pk_bf16_f32 v73, v74, v75\n"
  "v_cvt_pk_bf16_f32 v74, v76, v77\n"
  "v_cvt_pk_bf16_f32 v75, v78, v79\n"
  "v_cvt_pk_bf16_f32 v80, v80, v81\n"
  "v_cvt_pk_bf16_f32 v81, v82, v83\n"
  "v_cvt_pk_bf16_f32 v82, v84, v85\n"
  "v_cvt_pk_bf16_f32 v83, v86, v87\n"
  "v_cvt_pk_bf16_f32 v88, v88, v89\n"
  "v_cvt_pk_bf16_f32 v89, v90, v91\n"
  "v_cvt_pk_bf16_f32 v90, v92, v93\n"
  "v_cvt_pk_bf16_f32 v91, v94, v95\n"
  "s_add_u32 s88, s88, 4096\n"
  "buffer_store_dwordx4 v[64:67], v226, s[24:27], s88 offen offset:0 sc1\n"
  "buffer_store_dwordx4 v[72:75], v226, s[24:27], s88 offen offset:1024 sc1\n"
  "buffer_store_dwordx4 v[80:83], v226, s[24:27], s88 offen offset:2048 sc1\n"
  "buffer_store_dwordx4 v[88:91], v226, s[24:27], s88 offen offset:3072 sc1\n"
  "v_cvt_pk_bf16_f32 v96, v96, v97\n"
  "v_cvt_pk_bf16_f32 v97, v98, v99\n"
  "v_cvt_pk_bf16_f32 v98, v100, v101\n"
  "v_cvt_pk_bf16_f32 v99, v102, v103\n"
  "v_cvt_pk_bf16_f32 v104, v104, v105\n"
  "v_cvt_pk_bf16_f32 v105, v106, v107\n"
  "v_cvt_pk_bf16_f32 v106, v108, v109\n"
  "v_cvt_pk_bf16_f32 v107, v110, v111\n"
  "v_cvt_pk_bf16_f32 v112, v112, v113\n"
  "v_cvt_pk_bf16_f32 v113, v114, v115\n"
  "v_cvt_pk_bf16_f32 v114, v116, v117\n"
  "v_cvt_pk_bf16_f32 v115, v118, v119\n"
  "v_cvt_pk_bf16_f32 v120, v120, v121\n"
  "v_cvt_pk_bf16_f32 v121, v122, v123\n"
  "v_cvt_pk_bf16_f32 v122, v124, v125\n"
  "v_cvt_pk_bf16_f32 v123, v126, v127\n"
  "s_add_u32 s88, s88, 4096\n"
  "buffer_store_dwordx4 v[96:99], v226, s[24:27], s88 offen offset:0 sc1\n"
  "buffer_store_dwordx4 v[104:107], v226, s[24:27], s88 offen offset:1024 sc1\n"
  "buffer_store_dwordx4 v[112:115], v226, s[24:27], s88 offen offset:2048 sc1\n"
  "buffer_store_dwordx4 v[120:123], v226, s[24:27], s88 offen offset:3072 sc1\n"
  "s_add_u32 s88, s77, 69632\n"
  "s_cmp_eq_u32 s53, 0\n"
  "s_cselect_b32 s51, s88, 2146435072\n"
  "s_waitcnt vmcnt(0)\n"
  "s_barrier\n"
  "buffer_store_dwordx2 v[232:233], v227, s[24:27], s51 offen sc1\n"
  "s_branch L_exit\n"
  "L_loop3:\n"
  "v_mfma_f32_16x16x32_bf16 v[128:131], a[192:195], a[0:3], 0\n"
  "s_barrier\n"
  "ds_read_b64_tr_b16 a[224:225], v221 offset:0\n"
  "v_mfma_f32_16x16x32_bf16 v[128:131], a[196:199], a[4:7], v[128:131]\n"
  "ds_read_b64_tr_b16 a[226:227], v221 offset:4096\n"
  "v_fma_f32 v176, v176, s38, v210\n"
  "v_mfma_f32_16x16x32_bf16 v[128:131], a[200:203], a[8:11], v[128:131]\n"
  "v_exp_f32 v176, v176\n"
  "v_mfma_f32_16x16x32_bf16 v[128:131], a[204:207], a[12:15], v[128:131]\n"
  "ds_read_b64_tr_b16 a[228:229], v239 offset:0\n"
  "v_fma_f32 v177, v177, s38, v210\n"
  "v_mfma_f32_16x16x32_bf16 v[132:135], a[208:211], a[0:3], 0\n"
  "v_exp_f32 v177, v177\n"
  "v_mfma_f32_16x16x32_bf16 v[132:135], a[212:215], a[4:7], v[132:135]\n"
  "ds_read_b64_tr_b16 a[230:231], v239 offset:4096\n"
  "v_cvt_pk_bf16_f32 v176, v176, v177\n"
  "v_mfma_f32_16x16x32_bf16 v[132:135], a[216:219], a[8:11], v[132:135]\n"
  "ds_read_b64_tr_b16 a[232:233], v221 offset:512\n"
  "v_fma_f32 v178, v178, s38, v210\n"
  "v_mfma_f32_16x16x32_bf16 v[132:135], a[220:223], a[12:15], v[132:135]\n"
  "v_exp_f32 v178, v178\n"
  "v_mfma_f32_16x16x32_bf16 v[136:139], a[192:195], a[16:19], 0\n"
  "ds_read_b64_tr_b16 a[234:235], v221 offset:4608\n"
  "ds_read_b64_tr_b16 a[236:237], v239 offset:512\n"
  "v_mfma_f32_16x16x32_bf16 v[136:139], a[196:199], a[20:23], v[136:139]\n"
  "ds_read_b64_tr_b16 a[238:239], v239 offset:4608\n"
  "v_fma_f32 v179, v179, s38, v210\n"
  "v_mfma_f32_16x16x32_bf16 v[136:139], a[200:203], a[24:27], v[136:139]\n"
  "v_exp_f32 v179, v179\n"
  "v_mfma_f32_16x16x32_bf16 v[136:139], a[204:207], a[28:31], v[136:139]\n"
  "ds_read_b64_tr_b16 a[240:241], v221 offset:1024\n"
  "ds_read_b64_tr_b16 a[242:243], v221 offset:5120\n"
  "v_mfma_f32_16x16x32_bf16 v[140:143], a[208:211], a[16:19], 0\n"
  "ds_read_b64_tr_b16 a[244:245], v239 offset:1024\n"
  "v_cvt_pk_bf16_f32 v177, v178, v179\n"
  "v_mfma_f32_16x16x32_bf16 v[140:143], a[212:215], a[20:23], v[140:143]\n"
  "ds_read_b64_tr_b16 a[246:247], v239 offset:5120\n"
  "v_fma_f32 v180, v180, s38, v210\n"
  "v_mfma_f32_16x16x32_bf16 v[140:143], a[216:219], a[24:27], v[140:143]\n"
  "v_exp_f32 v180, v180\n"
  "v_mfma_f32_16x16x32_bf16 v[140:143], a[220:223], a[28:31], v[140:143]\n"
  "ds_read_b64_tr_b16 a[248:249], v221 offset:1536\n"
  "ds_read_b64_tr_b16 a[250:251], v221 offset:5632\n"
  "v_mfma_f32_16x16x32_bf16 v[144:147], a[192:195], a[32:35], 0\n"
  "ds_read_b64_tr_b16 a[252:253], v239 offset:1536\n"
  "v_fma_f32 v181, v181, s38, v210\n"
  "v_mfma_f32_16x16x32_bf16 v[144:147], a[196:199], a[36:39], v[144:147]\n"
  "ds_read_b64_tr_b16 a[254:255], v239 offset:5632\n"
  "v_fma_f32 v182, v182, s38, v210\n"
  "v_mfma_f32_16x16x32_bf16 v[144:147], a[200:203], a[40:43], v[144:147]\n"
  "v_exp_f32 v181, v181\n"
  "v_mfma_f32_16x16x32_bf16 v[144:147], a[204:207], a[44:47], v[144:147]\n"
  "v_cvt_pk_bf16_f32 v178, v180, v181\n"
  "v_fma_f32 v183, v183, s38, v210\n"
  "v_mfma_f32_16x16x32_bf16 v[148:151], a[208:211], a[32:35], 0\n"
  "v_exp_f32 v182, v182\n"
  "v_mfma_f32_16x16x32_bf16 v[148:151], a[212:215], a[36:39], v[148:151]\n"
  "v_exp_f32 v183, v183\n"
  "v_mfma_f32_16x16x32_bf16 v[148:151], a[216:219], a[40:43], v[148:151]\n"
  "v_cvt_pk_bf16_f32 v179, v182, v183\n"
  "v_mfma_f32_16x16x32_bf16 v[148:151], a[220:223], a[44:47], v[148:151]\n"
  "s_waitcnt lgkmcnt(0)\n"
  "v_mfma_f32_16x16x32_bf16 v[192:195], v[216:219], v[160:163], v[192:195]\n"
  "ds_read_b128 a[192:195], v220 offset:0\n"
  "v_fma_f32 v128, v128, s38, v208\n"
  "v_fma_f32 v129, v129, s38, v208\n"
  "v_mfma_f32_16x16x32_bf16 v[0:3], a[224:227], v[160:163], v[0:3]\n"
  "v_exp_f32 v128, v128\n"
  "v_mfma_f32_16x16x32_bf16 v[4:7], a[228:231], v[160:163], v[4:7]\n"
  "ds_read_b128 a[196:199], v220 offset:512\n"
  "v_exp_f32 v129, v129\n"
  "v_mfma_f32_16x16x32_bf16 v[8:11], a[232:235], v[160:163], v[8:11]\n"
  "ds_read_b128 a[200:203], v220 offset:1024\n"
  "v_cvt_pk_bf16_f32 v128, v128, v129\n"
  "v_mfma_f32_16x16x32_bf16 v[12:15], a[236:239], v[160:163], v[12:15]\n"
  "ds_read_b128 a[204:207], v220 offset:1536\n"
  "v_fma_f32 v130, v130, s38, v208\n"
  "v_fma_f32 v131, v131, s38, v208\n"
  "v_mfma_f32_16x16x32_bf16 v[16:19], a[240:243], v[160:163], v[16:19]\n"
  "v_exp_f32 v130, v130\n"
  "v_mfma_f32_16x16x32_bf16 v[20:23], a[244:247], v[160:163], v[20:23]\n"
  "ds_read_b128 a[208:211], v220 offset:4096\n"
  "v_exp_f32 v131, v131\n"
  "v_mfma_f32_16x16x32_bf16 v[24:27], a[248:251], v[160:163], v[24:27]\n"
  "ds_read_b128 a[212:215], v220 offset:4608\n"
  "v_cvt_pk_bf16_f32 v129, v130, v131\n"
  "v_mfma_f32_16x16x32_bf16 v[28:31], a[252:255], v[160:163], v[28:31]\n"
  "ds_read_b128 a[216:219], v220 offset:5120\n"
  "v_fma_f32 v132, v132, s38, v208\n"
  "v_fma_f32 v133, v133, s38, v208\n"
  "v_mfma_f32_16x16x32_bf16 v[196:199], v[216:219], v[168:171], v[196:199]\n"
  "v_exp_f32 v132, v132\n"
  "v_mfma_f32_16x16x32_bf16 v[32:35], a[224:227], v[168:171], v[32:35]\n"
  "ds_read_b128 a[220:223], v220 offset:5632\n"
  "v_exp_f32 v133, v133\n"
  "v_mfma_f32_16x16x32_bf16 v[36:39], a[228:231], v[168:171], v[36:39]\n"
  "v_cvt_pk_bf16_f32 v130, v132, v133\n"
  "v_fma_f32 v134, v134, s38, v208\n"
  "v_mfma_f32_16x16x32_bf16 v[40:43], a[232:235], v[168:171], v[40:43]\n"
  "buffer_store_dwordx2 v[232:233], v227, s[24:27], s51 offen sc1\n"
  "v_fma_f32 v135, v135, s38, v208\n"
  "v_mfma_f32_16x16x32_bf16 v[44:47], a[236:239], v[168:171], v[44:47]\n"
  "v_exp_f32 v134, v134\n"
  "v_mfma_f32_16x16x32_bf16 v[48:51], a[240:243], v[168:171], v[48:51]\n"
  "v_exp_f32 v135, v135\n"
  "v_fma_f32 v136, v136, s38, v209\n"
  "v_mfma_f32_16x16x32_bf16 v[52:55], a[244:247], v[168:171], v[52:55]\n"
  "v_cvt_pk_bf16_f32 v131, v134, v135\n"
  "v_fma_f32 v137, v137, s38, v209\n"
  "v_mfma_f32_16x16x32_bf16 v[56:59], a[248:251], v[168:171], v[56:59]\n"
  "v_exp_f32 v136, v136\n"
  "v_fma_f32 v138, v138, s38, v209\n"
  "v_mfma_f32_16x16x32_bf16 v[60:63], a[252:255], v[168:171], v[60:63]\n"
  "buffer_load_dwordx2 v[230:231], v227, s[24:27], s50 offen sc1\n"
  "v_mfma_f32_16x16x32_bf16 v[200:203], v[216:219], v[176:179], v[200:203]\n"
  "v_exp_f32 v137, v137\n"
  "v_fma_f32 v139, v139, s38, v209\n"
  "v_mfma_f32_16x16x32_bf16 v[64:67], a[224:227], v[176:179], v[64:67]\n"
  "v_cvt_pk_bf16_f32 v136, v136, v137\n"
  "v_fma_f32 v140, v140, s38, v209\n"
  "v_mfma_f32_16x16x32_bf16 v[68:71], a[228:231], v[176:179], v[68:71]\n"
  "v_exp_f32 v138, v138\n"
  "v_fma_f32 v141, v141, s38, v209\n"
  "v_mfma_f32_16x16x32_bf16 v[72:75], a[232:235], v[176:179], v[72:75]\n"
  "v_exp_f32 v139, v139\n"
  "v_mfma_f32_16x16x32_bf16 v[76:79], a[236:239], v[176:179], v[76:79]\n"
  "v_cvt_pk_bf16_f32 v137, v138, v139\n"
  "v_exp_f32 v140, v140\n"
  "v_mfma_f32_16x16x32_bf16 v[80:83], a[240:243], v[176:179], v[80:83]\n"
  "v_exp_f32 v141, v141\n"
  "v_mfma_f32_16x16x32_bf16 v[84:87], a[244:247], v[176:179], v[84:87]\n"
  "v_cvt_pk_bf16_f32 v138, v140, v141\n"
  "v_fma_f32 v142, v142, s38, v209\n"
  "v_fma_f32 v143, v143, s38, v209\n"
  "v_mfma_f32_16x16x32_bf16 v[88:91], a[248:251], v[176:179], v[88:91]\n"
  "v_exp_f32 v142, v142\n"
  "v_mfma_f32_16x16x32_bf16 v[92:95], a[252:255], v[176:179], v[92:95]\n"
  "v_exp_f32 v143, v143\n"
  "s_waitcnt lgkmcnt(0)\n"
  "v_cvt_pk_bf16_f32 v139, v142, v143\n"
  "L_loop3_G2:\n"
  "v_mfma_f32_16x16x32_bf16 v[160:163], a[192:195], a[0:3], 0\n"
  "ds_read_b64_tr_b16 a[224:225], v221 offset:8192\n"
  "v_fma_f32 v144, v144, s38, v210\n"
  "v_mfma_f32_16x16x32_bf16 v[160:163], a[196:199], a[4:7], v[160:163]\n"
  "ds_read_b64_tr_b16 a[226:227], v221 offset:12288\n"
  "v_exp_f32 v144, v144\n"
  "v_mfma_f32_16x16x32_bf16 v[160:163], a[200:203], a[8:11], v[160:163]\n"
  "ds_read_b64_tr_b16 a[228:229], v239 offset:8192\n"
  "v_fma_f32 v145, v145, s38, v210\n"
  "v_mfma_f32_16x16x32_bf16 v[160:163], a[204:207], a[12:15], v[160:163]\n"
  "s_or_b32 m0, s45, s43\n"
  "v_exp_f32 v145, v145\n"
  "v_mfma_f32_16x16x32_bf16 v[164:167], a[208:211], a[0:3], 0\n"
  "buffer_load_dwordx4 v222, s[8:11], s39 offen offset:0 lds\n"
  "v_mfma_f32_16x16x32_bf16 v[164:167], a[212:215], a[4:7], v[164:167]\n"
  "ds_read_b64_tr_b16 a[230:231], v239 offset:12288\n"
  "v_cvt_pk_bf16_f32 v144, v144, v145\n"
  "v_mfma_f32_16x16x32_bf16 v[164:167], a[216:219], a[8:11], v[164:167]\n"
  "ds_read_b64_tr_b16 a[232:233], v221 offset:8704\n"
  "v_fma_f32 v146, v146, s38, v210\n"
  "v_fma_f32 v147, v147, s38, v210\n"
  "v_mfma_f32_16x16x32_bf16 v[164:167], a[220:223], a[12:15], v[164:167]\n"
  "v_exp_f32 v146, v146\n"
  "v_mfma_f32_16x16x32_bf16 v[168:171], a[192:195], a[16:19], 0\n"
  "ds_read_b64_tr_b16 a[234:235], v221 offset:12800\n"
  "v_exp_f32 v147, v147\n"
  "v_mfma_f32_16x16x32_bf16 v[168:171], a[196:199], a[20:23], v[168:171]\n"
  "buffer_load_dwordx4 v223, s[8:11], s39 offen offset:1024 lds\n"
  "v_mfma_f32_16x16x32_bf16 v[168:171], a[200:203], a[24:27], v[168:171]\n"
  "ds_read_b64_tr_b16 a[236:237], v239 offset:8704\n"
  "ds_read_b64_tr_b16 a[238:239], v239 offset:12800\n"
  "v_cvt_pk_bf16_f32 v145, v146, v147\n"
  "v_mfma_f32_16x16x32_bf16 v[168:171], a[204:207], a[28:31], v[168:171]\n"
  "ds_read_b64_tr_b16 a[240:241], v221 offset:9216\n"
  "ds_read_b64_tr_b16 a[242:243], v221 offset:13312\n"
  "v_mfma_f32_16x16x32_bf16 v[172:175], a[208:211], a[16:19], 0\n"
  "ds_read_b64_tr_b16 a[244:245], v239 offset:9216\n"
  "v_fma_f32 v148, v148, s38, v210\n"
  "v_mfma_f32_16x16x32_bf16 v[172:175], a[212:215], a[20:23], v[172:175]\n"
  "buffer_load_dwordx4 v224, s[8:11], s39 offen offset:2048 lds\n"
  "v_fma_f32 v149, v149, s38, v210\n"
  "v_mfma_f32_16x16x32_bf16 v[172:175], a[216:219], a[24:27], v[172:175]\n"
  "v_exp_f32 v148, v148\n"
  "v_mfma_f32_16x16x32_bf16 v[172:175], a[220:223], a[28:31], v[172:175]\n"
  "ds_read_b64_tr_b16 a[246:247], v239 offset:13312\n"
  "v_exp_f32 v149, v149\n"
  "v_mfma_f32_16x16x32_bf16 v[176:179], a[192:195], a[32:35], 0\n"
  "ds_read_b64_tr_b16 a[248:249], v221 offset:9728\n"
  "ds_read_b64_tr_b16 a[250:251], v221 offset:13824\n"
  "v_mfma_f32_16x16x32_bf16 v[176:179], a[196:199], a[36:39], v[176:179]\n"
  "buffer_load_dwordx4 v225, s[8:11], s39 offen offset:3072 lds\n"
  "ds_read_b64_tr_b16 a[252:253], v239 offset:9728\n"
  "ds_read_b64_tr_b16 a[254:255], v239 offset:13824\n"
  "v_mfma_f32_16x16x32_bf16 v[176:179], a[200:203], a[40:43], v[176:179]\n"
  "v_xor_b32 v221, s43, v221\n"
  "v_cvt_pk_bf16_f32 v146, v148, v149\n"
  "v_mfma_f32_16x16x32_bf16 v[176:179], a[204:207], a[44:47], v[176:179]\n"
  "v_xor_b32 v239, s43, v239\n"
  "v_fma_f32 v150, v150, s38, v210\n"
  "v_mfma_f32_16x16x32_bf16 v[180:183], a[208:211], a[32:35], 0\n"
  "v_exp_f32 v150, v150\n"
  "v_mfma_f32_16x16x32_bf16 v[180:183], a[212:215], a[36:39], v[180:183]\n"
  "s_or_b32 m0, s46, s43\n"
  "v_fma_f32 v151, v151, s38, v210\n"
  "v_mfma_f32_16x16x32_bf16 v[180:183], a[216:219], a[40:43], v[180:183]\n"
  "buffer_load_dwordx4 v222, s[12:15], s41 offen offset:0 lds\n"
  "v_mfma_f32_16x16x32_bf16 v[180:183], a[220:223], a[44:47], v[180:183]\n"
  "v_exp_f32 v151, v151\n"
  "s_waitcnt lgkmcnt(0)\n"
  "v_cvt_pk_bf16_f32 v147, v150, v151\n"
  "v_mfma_f32_16x16x32_bf16 v[192:195], v[212:215], v[128:131], v[192:195]\n"
  "ds_read_b128 a[192:195], v220 offset:8192\n"
  "v_fma_f32 v160, v160, s38, v208\n"
  "v_fma_f32 v161, v161, s38, v208\n"
  "v_mfma_f32_16x16x32_bf16 v[0:3], a[224:227], v[128:131], v[0:3]\n"
  "ds_read_b128 a[196:199], v220 offset:8704\n"
  "v_exp_f32 v160, v160\n"
  "v_mfma_f32_16x16x32_bf16 v[4:7], a[228:231], v[128:131], v[4:7]\n"
  "buffer_load_dwordx4 v223, s[12:15], s41 offen offset:1024 lds\n"
  "v_fma_f32 v162, v162, s38, v208\n"
  "v_mfma_f32_16x16x32_bf16 v[8:11], a[232:235], v[128:131], v[8:11]\n"
  "v_exp_f32 v161, v161\n"
  "v_mfma_f32_16x16x32_bf16 v[12:15], a[236:239], v[128:131], v[12:15]\n"
  "ds_read_b128 a[200:203], v220 offset:9216\n"
  "v_cvt_pk_bf16_f32 v160, v160, v161\n"
  "v_fma_f32 v163, v163, s38, v208\n"
  "v_mfma_f32_16x16x32_bf16 v[16:19], a[240:243], v[128:131], v[16:19]\n"
  "ds_read_b128 a[204:207], v220 offset:9728\n"
  "v_exp_f32 v162, v162\n"
  "v_mfma_f32_16x16x32_bf16 v[20:23], a[244:247], v[128:131], v[20:23]\n"
  "ds_read_b128 a[208:211], v220 offset:12288\n"
  "v_exp_f32 v163, v163\n"
  "v_mfma_f32_16x16x32_bf16 v[24:27], a[248:251], v[128:131], v[24:27]\n"
  "buffer_load_dwordx4 v224, s[12:15], s41 offen offset:2048 lds\n"
  "ds_read_b128 a[212:215], v220 offset:12800\n"
  "v_mfma_f32_16x16x32_bf16 v[28:31], a[252:255], v[128:131], v[28:31]\n"
  "ds_read_b128 a[216:219], v220 offset:13312\n"
  "v_cvt_pk_bf16_f32 v161, v162, v163\n"
  "v_fma_f32 v164, v164, s38, v208\n"
  "v_mfma_f32_16x16x32_bf16 v[196:199], v[212:215], v[136:139], v[196:199]\n"
  "ds_read_b128 a[220:223], v220 offset:13824\n"
  "v_exp_f32 v164, v164\n"
  "v_mfma_f32_16x16x32_bf16 v[32:35], a[224:227], v[136:139], v[32:35]\n"
  "v_xor_b32 v220, s43, v220\n"
  "v_fma_f32 v165, v165, s38, v208\n"
  "v_mfma_f32_16x16x32_bf16 v[36:39], a[228:231], v[136:139], v[36:39]\n"
  "buffer_load_dwordx4 v225, s[12:15], s41 offen offset:3072 lds\n"
  "v_fma_f32 v166, v166, s38, v208\n"
  "v_mfma_f32_16x16x32_bf16 v[40:43], a[232:235], v[136:139], v[40:43]\n"
  "v_exp_f32 v165, v165\n"
  "v_fma_f32 v167, v167, s38, v208\n"
  "v_mfma_f32_16x16x32_bf16 v[44:47], a[236:239], v[136:139], v[44:47]\n"
  "v_cvt_pk_bf16_f32 v162, v164, v165\n"
  "v_exp_f32 v166, v166\n"
  "v_mfma_f32_16x16x32_bf16 v[48:51], a[240:243], v[136:139], v[48:51]\n"
  "v_exp_f32 v167, v167\n"
  "v_fma_f32 v168, v168, s38, v209\n"
  "v_mfma_f32_16x16x32_bf16 v[52:55], a[244:247], v[136:139], v[52:55]\n"
  "v_cvt_pk_bf16_f32 v163, v166, v167\n"
  "v_exp_f32 v168, v168\n"
  "v_mfma_f32_16x16x32_bf16 v[56:59], a[248:251], v[136:139], v[56:59]\n"
  "v_fma_f32 v169, v169, s38, v209\n"
  "v_fma_f32 v170, v170, s38, v209\n"
  "v_mfma_f32_16x16x32_bf16 v[60:63], a[252:255], v[136:139], v[60:63]\n"
  "v_exp_f32 v169, v169\n"
  "v_fma_f32 v171, v171, s38, v209\n"
  "v_mfma_f32_16x16x32_bf16 v[200:203], v[212:215], v[144:147], v[200:203]\n"
  "v_cvt_pk_bf16_f32 v168, v168, v169\n"
  "v_exp_f32 v170, v170\n"
  "v_mfma_f32_16x16x32_bf16 v[64:67], a[224:227], v[144:147], v[64:67]\n"
  "v_exp_f32 v171, v171\n"
  "v_fma_f32 v172, v172, s38, v209\n"
  "v_mfma_f32_16x16x32_bf16 v[68:71], a[228:231], v[144:147], v[68:71]\n"
  "v_cvt_pk_bf16_f32 v169, v170, v171\n"
  "v_exp_f32 v172, v172\n"
  "v_mfma_f32_16x16x32_bf16 v[72:75], a[232:235], v[144:147], v[72:75]\n"
  "s_mov_b32 s41, s39\n"
  "v_fma_f32 v173, v173, s38, v209\n"
  "v_fma_f32 v174, v174, s38, v209\n"
  "v_mfma_f32_16x16x32_bf16 v[76:79], a[236:239], v[144:147], v[76:79]\n"
  "s_mov_b32 s14, s10\n"
  "v_exp_f32 v173, v173\n"
  "v_mfma_f32_16x16x32_bf16 v[80:83], a[240:243], v[144:147], v[80:83]\n"
  "s_add_u32 s39, s39, 0x40000\n"
  "v_cvt_pk_bf16_f32 v170, v172, v173\n"
  "v_mfma_f32_16x16x32_bf16 v[84:87], a[244:247], v[144:147], v[84:87]\n"
  "s_xor_b32 s42, s42, s43\n"
  "v_exp_f32 v174, v174\n"
  "v_mfma_f32_16x16x32_bf16 v[88:91], a[248:251], v[144:147], v[88:91]\n"
  "s_xor_b32 s43, s42, s43\n"
  "v_fma_f32 v175, v175, s38, v209\n"
  "v_mfma_f32_16x16x32_bf16 v[92:95], a[252:255], v[144:147], v[92:95]\n"
  "s_sub_u32 s44, s44, 1\n"
  "v_exp_f32 v175, v175\n"
  "s_waitcnt vmcnt(8) lgkmcnt(0)\n"
  "v_cvt_pk_bf16_f32 v171, v174, v175\n"
  "s_cbranch_scc0 L_loop3\n"
  "s_branch L_disp\n"
  "L_loop2:\n"
  "v_mfma_f32_16x16x32_bf16 v[128:131], a[192:195], a[0:3], 0\n"
  "s_barrier\n"
  "ds_read_b64_tr_b16 a[224:225], v221 offset:0\n"
  "ds_read_b64_tr_b16 a[226:227], v221 offset:4096\n"
  "v_mfma_f32_16x16x32_bf16 v[128:131], a[196:199], a[4:7], v[128:131]\n"
  "ds_read_b64_tr_b16 a[228:229], v239 offset:0\n"
  "ds_read_b64_tr_b16 a[230:231], v239 offset:4096\n"
  "v_fma_f32 v176, v176, s38, v210\n"
  "v_fma_f32 v177, v177, s38, v210\n"
  "v_mfma_f32_16x16x32_bf16 v[128:131], a[200:203], a[8:11], v[128:131]\n"
  "ds_read_b64_tr_b16 a[232:233], v221 offset:512\n"
  "v_exp_f32 v176, v176\n"
  "v_mfma_f32_16x16x32_bf16 v[128:131], a[204:207], a[12:15], v[128:131]\n"
  "buffer_store_dwordx2 v[232:233], v227, s[24:27], s51 offen sc1\n"
  "v_exp_f32 v177, v177\n"
  "v_mfma_f32_16x16x32_bf16 v[132:135], a[208:211], a[0:3], 0\n"
  "ds_read_b64_tr_b16 a[234:235], v221 offset:4608\n"
  "ds_read_b64_tr_b16 a[236:237], v239 offset:512\n"
  "v_cvt_pk_bf16_f32 v176, v176, v177\n"
  "v_mfma_f32_16x16x32_bf16 v[132:135], a[212:215], a[4:7], v[132:135]\n"
  "ds_read_b64_tr_b16 a[238:239], v239 offset:4608\n"
  "ds_read_b64_tr_b16 a[240:241], v221 offset:1024\n"
  "v_fma_f32 v178, v178, s38, v210\n"
  "v_mfma_f32_16x16x32_bf16 v[132:135], a[216:219], a[8:11], v[132:135]\n"
  "buffer_load_dwordx2 v[230:231], v227, s[24:27], s50 offen sc1\n"
  "v_exp_f32 v178, v178\n"
  "v_mfma_f32_16x16x32_bf16 v[132:135], a[220:223], a[12:15], v[132:135]\n"
  "ds_read_b64_tr_b16 a[242:243], v221 offset:5120\n"
  "ds_read_b64_tr_b16 a[244:245], v239 offset:1024\n"
  "v_fma_f32 v179, v179, s38, v210\n"
  "v_mfma_f32_16x16x32_bf16 v[144:147], a[192:195], a[32:35], 0\n"
  "s_or_b32 m0, s45, s43\n"
  "v_exp_f32 v179, v179\n"
  "v_mfma_f32_16x16x32_bf16 v[144:147], a[196:199], a[36:39], v[144:147]\n"
  "buffer_load_dwordx4 v222, s[8:11], s39 offen offset:0 lds\n"
  "ds_read_b64_tr_b16 a[246:247], v239 offset:5120\n"
  "ds_read_b64_tr_b16 a[248:249], v221 offset:1536\n"
  "v_mfma_f32_16x16x32_bf16 v[144:147], a[200:203], a[40:43], v[144:147]\n"
  "ds_read_b64_tr_b16 a[250:251], v221 offset:5632\n"
  "ds_read_b64_tr_b16 a[252:253], v239 offset:1536\n"
  "v_cvt_pk_bf16_f32 v177, v178, v179\n"
  "v_mfma_f32_16x16x32_bf16 v[144:147], a[204:207], a[44:47], v[144:147]\n"
  "ds_read_b64_tr_b16 a[254:255], v239 offset:5632\n"
  "v_fma_f32 v180, v180, s38, v210\n"
  "v_fma_f32 v181, v181, s38, v210\n"
  "v_fma_f32 v182, v182, s38, v210\n"
  "v_mfma_f32_16x16x32_bf16 v[148:151], a[208:211], a[32:35], 0\n"
  "v_exp_f32 v180, v180\n"
  "v_fma_f32 v183, v183, s38, v210\n"
  "v_mfma_f32_16x16x32_bf16 v[148:151], a[212:215], a[36:39], v[148:151]\n"
  "buffer_load_dwordx4 v223, s[8:11], s39 offen offset:1024 lds\n"
  "v_mfma_f32_16x16x32_bf16 v[148:151], a[216:219], a[40:43], v[148:151]\n"
  "v_exp_f32 v181, v181\n"
  "v_exp_f32 v182, v182\n"
  "v_mfma_f32_16x16x32_bf16 v[148:151], a[220:223], a[44:47], v[148:151]\n"
  "v_cvt_pk_bf16_f32 v178, v180, v181\n"
  "v_exp_f32 v183, v183\n"
  "s_waitcnt lgkmcnt(0)\n"
  "v_cvt_pk_bf16_f32 v179, v182, v183\n"
  "v_mfma_f32_16x16x32_bf16 v[192:195], v[216:219], v[160:163], v[192:195]\n"
  "buffer_load_dwordx4 v224, s[8:11], s39 offen offset:2048 lds\n"
  "ds_read_b128 a[192:195], v220 offset:0\n"
  "v_mfma_f32_16x16x32_bf16 v[0:3], a[224:227], v[160:163], v[0:3]\n"
  "ds_read_b128 a[196:199], v220 offset:512\n"
  "v_fma_f32 v128, v128, s38, v208\n"
  "v_mfma_f32_16x16x32_bf16 v[4:7], a[228:231], v[160:163], v[4:7]\n"
  "ds_read_b128 a[200:203], v220 offset:1024\n"
  "v_exp_f32 v128, v128\n"
  "v_mfma_f32_16x16x32_bf16 v[8:11], a[232:235], v[160:163], v[8:11]\n"
  "buffer_load_dwordx4 v225, s[8:11], s39 offen offset:3072 lds\n"
  "ds_read_b128 a[204:207], v220 offset:1536\n"
  "v_mfma_f32_16x16x32_bf16 v[12:15], a[236:239], v[160:163], v[12:15]\n"
  "ds_read_b128 a[208:211], v220 offset:4096\n"
  "v_fma_f32 v129, v129, s38, v208\n"
  "v_fma_f32 v130, v130, s38, v208\n"
  "v_mfma_f32_16x16x32_bf16 v[16:19], a[240:243], v[160:163], v[16:19]\n"
  "s_or_b32 m0, s46, s43\n"
  "v_fma_f32 v131, v131, s38, v208\n"
  "v_mfma_f32_16x16x32_bf16 v[20:23], a[244:247], v[160:163], v[20:23]\n"
  "buffer_load_dwordx4 v222, s[12:15], s41 offen offset:0 lds\n"
  "v_fma_f32 v132, v132, s38, v208\n"
  "v_mfma_f32_16x16x32_bf16 v[24:27], a[248:251], v[160:163], v[24:27]\n"
  "ds_read_b128 a[212:215], v220 offset:4608\n"
  "v_exp_f32 v129, v129\n"
  "v_mfma_f32_16x16x32_bf16 v[28:31], a[252:255], v[160:163], v[28:31]\n"
  "v_cvt_pk_bf16_f32 v128, v128, v129\n"
  "v_fma_f32 v133, v133, s38, v208\n"
  "v_mfma_f32_16x16x32_bf16 v[200:203], v[216:219], v[176:179], v[200:203]\n"
  "buffer_load_dwordx4 v223, s[12:15], s41 offen offset:1024 lds\n"
  "ds_read_b128 a[216:219], v220 offset:5120\n"
  "ds_read_b128 a[220:223], v220 offset:5632\n"
  "v_mfma_f32_16x16x32_bf16 v[64:67], a[224:227], v[176:179], v[64:67]\n"
  "v_exp_f32 v130, v130\n"
  "v_mfma_f32_16x16x32_bf16 v[68:71], a[228:231], v[176:179], v[68:71]\n"
  "v_exp_f32 v131, v131\n"
  "v_fma_f32 v134, v134, s38, v208\n"
  "v_mfma_f32_16x16x32_bf16 v[72:75], a[232:235], v[176:179], v[72:75]\n"
  "v_cvt_pk_bf16_f32 v129, v130, v131\n"
  "v_fma_f32 v135, v135, s38, v208\n"
  "v_mfma_f32_16x16x32_bf16 v[76:79], a[236:239], v[176:179], v[76:79]\n"
  "buffer_load_dwordx4 v224, s[12:15], s41 offen offset:2048 lds\n"
  "v_mfma_f32_16x16x32_bf16 v[80:83], a[240:243], v[176:179], v[80:83]\n"
  "v_exp_f32 v132, v132\n"
  "v_exp_f32 v133, v133\n"
  "v_mfma_f32_16x16x32_bf16 v[84:87], a[244:247], v[176:179], v[84:87]\n"
  "v_cvt_pk_bf16_f32 v130, v132, v133\n"
  "v_exp_f32 v134, v134\n"
  "v_mfma_f32_16x16x32_bf16 v[88:91], a[248:251], v[176:179], v[88:91]\n"
  "buffer_load_dwordx4 v225, s[12:15], s41 offen offset:3072 lds\n"
  "v_mfma_f32_16x16x32_bf16 v[92:95], a[252:255], v[176:179], v[92:95]\n"
  "v_exp_f32 v135, v135\n"
  "s_waitcnt lgkmcnt(0)\n"
  "v_cvt_pk_bf16_f32 v131, v134, v135\n"
  "L_loop2_G2:\n"
  "v_mfma_f32_16x16x32_bf16 v[160:163], a[192:195], a[0:3], 0\n"
  "ds_read_b64_tr_b16 a[224:225], v221 offset:8192\n"
  "ds_read_b64_tr_b16 a[226:227], v221 offset:12288\n"
  "v_fma_f32 v144, v144, s38, v210\n"
  "v_mfma_f32_16x16x32_bf16 v[160:163], a[196:199], a[4:7], v[160:163]\n"
  "ds_read_b64_tr_b16 a[228:229], v239 offset:8192\n"
  "v_exp_f32 v144, v144\n"
  "v_mfma_f32_16x16x32_bf16 v[160:163], a[200:203], a[8:11], v[160:163]\n"
  "ds_read_b64_tr_b16 a[230:231], v239 offset:12288\n"
  "ds_read_b64_tr_b16 a[232:233], v221 offset:8704\n"
  "v_fma_f32 v145, v145, s38, v210\n"
  "v_mfma_f32_16x16x32_bf16 v[160:163], a[204:207], a[12:15], v[160:163]\n"
  "ds_read_b64_tr_b16 a[234:235], v221 offset:12800\n"
  "v_exp_f32 v145, v145\n"
  "v_mfma_f32_16x16x32_bf16 v[164:167], a[208:211], a[0:3], 0\n"
  "ds_read_b64_tr_b16 a[236:237], v239 offset:8704\n"
  "ds_read_b64_tr_b16 a[238:239], v239 offset:12800\n"
  "v_cvt_pk_bf16_f32 v144, v144, v145\n"
  "v_mfma_f32_16x16x32_bf16 v[164:167], a[212:215], a[4:7], v[164:167]\n"
  "ds_read_b64_tr_b16 a[240:241], v221 offset:9216\n"
  "ds_read_b64_tr_b16 a[242:243], v221 offset:13312\n"
  "v_fma_f32 v146, v146, s38, v210\n"
  "v_mfma_f32_16x16x32_bf16 v[164:167], a[216:219], a[8:11], v[164:167]\n"
  "ds_read_b64_tr_b16 a[244:245], v239 offset:9216\n"
  "v_exp_f32 v146, v146\n"
  "v_mfma_f32_16x16x32_bf16 v[164:167], a[220:223], a[12:15], v[164:167]\n"
  "ds_read_b64_tr_b16 a[246:247], v239 offset:13312\n"
  "v_fma_f32 v147, v147, s38, v210\n"
  "v_fma_f32 v148, v148, s38, v210\n"
  "v_fma_f32 v149, v149, s38, v210\n"
  "v_mfma_f32_16x16x32_bf16 v[176:179], a[192:195], a[32:35], 0\n"
  "ds_read_b64_tr_b16 a[248:249], v221 offset:9728\n"
  "v_exp_f32 v147, v147\n"
  "v_mfma_f32_16x16x32_bf16 v[176:179], a[196:199], a[36:39], v[176:179]\n"
  "ds_read_b64_tr_b16 a[250:251], v221 offset:13824\n"
  "v_cvt_pk_bf16_f32 v145, v146, v147\n"
  "v_fma_f32 v150, v150, s38, v210\n"
  "v_mfma_f32_16x16x32_bf16 v[176:179], a[200:203], a[40:43], v[176:179]\n"
  "ds_read_b64_tr_b16 a[252:253], v239 offset:9728\n"
  "v_exp_f32 v148, v148\n"
  "v_mfma_f32_16x16x32_bf16 v[176:179], a[204:207], a[44:47], v[176:179]\n"
  "ds_read_b64_tr_b16 a[254:255], v239 offset:13824\n"
  "v_exp_f32 v149, v149\n"
  "v_mfma_f32_16x16x32_bf16 v[180:183], a[208:211], a[32:35], 0\n"
  "v_cvt_pk_bf16_f32 v146, v148, v149\n"
  "v_exp_f32 v150, v150\n"
  "v_mfma_f32_16x16x32_bf16 v[180:183], a[212:215], a[36:39], v[180:183]\n"
  "s_mov_b32 s41, s39\n"
  "s_mov_b32 s14, s10\n"
  "v_fma_f32 v151, v151, s38, v210\n"
  "v_mfma_f32_16x16x32_bf16 v[180:183], a[216:219], a[40:43], v[180:183]\n"
  "v_exp_f32 v151, v151\n"
  "s_add_u32 s39, s39, 0x40000\n"
  "v_mfma_f32_16x16x32_bf16 v[180:183], a[220:223], a[44:47], v[180:183]\n"
  "v_xor_b32 v221, s43, v221\n"
  "v_xor_b32 v239, s43, v239\n"
  "v_cvt_pk_bf16_f32 v147, v150, v151\n"
  "s_waitcnt lgkmcnt(0)\n"
  "v_mfma_f32_16x16x32_bf16 v[192:195], v[212:215], v[128:131], v[192:195]\n"
  "ds_read_b128 a[192:195], v220 offset:8192\n"
  "v_fma_f32 v160, v160, s38, v208\n"
  "v_mfma_f32_16x16x32_bf16 v[0:3], a[224:227], v[128:131], v[0:3]\n"
  "v_exp_f32 v160, v160\n"
  "v_mfma_f32_16x16x32_bf16 v[4:7], a[228:231], v[128:131], v[4:7]\n"
  "ds_read_b128 a[196:199], v220 offset:8704\n"
  "ds_read_b128 a[200:203], v220 offset:9216\n"
  "v_fma_f32 v161, v161, s38, v208\n"
  "v_mfma_f32_16x16x32_bf16 v[8:11], a[232:235], v[128:131], v[8:11]\n"
  "v_exp_f32 v161, v161\n"
  "v_mfma_f32_16x16x32_bf16 v[12:15], a[236:239], v[128:131], v[12:15]\n"
  "ds_read_b128 a[204:207], v220 offset:9728\n"
  "ds_read_b128 a[208:211], v220 offset:12288\n"
  "v_mfma_f32_16x16x32_bf16 v[16:19], a[240:243], v[128:131], v[16:19]\n"
  "ds_read_b128 a[212:215], v220 offset:12800\n"
  "v_cvt_pk_bf16_f32 v160, v160, v161\n"
  "v_mfma_f32_16x16x32_bf16 v[20:23], a[244:247], v[128:131], v[20:23]\n"
  "ds_read_b128 a[216:219], v220 offset:13312\n"
  "v_fma_f32 v162, v162, s38, v208\n"
  "v_fma_f32 v163, v163, s38, v208\n"
  "v_mfma_f32_16x16x32_bf16 v[24:27], a[248:251], v[128:131], v[24:27]\n"
  "v_exp_f32 v162, v162\n"
  "v_mfma_f32_16x16x32_bf16 v[28:31], a[252:255], v[128:131], v[28:31]\n"
  "ds_read_b128 a[220:223], v220 offset:13824\n"
  "v_xor_b32 v220, s43, v220\n"
  "v_mfma_f32_16x16x32_bf16 v[200:203], v[212:215], v[144:147], v[200:203]\n"
  "v_exp_f32 v163, v163\n"
  "v_mfma_f32_16x16x32_bf16 v[64:67], a[224:227], v[144:147], v[64:67]\n"
  "v_cvt_pk_bf16_f32 v161, v162, v163\n"
  "v_fma_f32 v164, v164, s38, v208\n"
  "v_mfma_f32_16x16x32_bf16 v[68:71], a[228:231], v[144:147], v[68:71]\n"
  "v_exp_f32 v164, v164\n"
  "v_fma_f32 v165, v165, s38, v208\n"
  "v_mfma_f32_16x16x32_bf16 v[72:75], a[232:235], v[144:147], v[72:75]\n"
  "v_exp_f32 v165, v165\n"
  "v_mfma_f32_16x16x32_bf16 v[76:79], a[236:239], v[144:147], v[76:79]\n"
  "v_cvt_pk_bf16_f32 v162, v164, v165\n"
  "v_fma_f32 v166, v166, s38, v208\n"
  "v_mfma_f32_16x16x32_bf16 v[80:83], a[240:243], v[144:147], v[80:83]\n"
  "v_exp_f32 v166, v166\n"
  "v_mfma_f32_16x16x32_bf16 v[84:87], a[244:247], v[144:147], v[84:87]\n"
  "s_xor_b32 s42, s42, s43\n"
  "v_fma_f32 v167, v167, s38, v208\n"
  "v_mfma_f32_16x16x32_bf16 v[88:91], a[248:251], v[144:147], v[88:91]\n"
  "s_xor_b32 s43, s42, s43\n"
  "v_exp_f32 v167, v167\n"
  "v_mfma_f32_16x16x32_bf16 v[92:95], a[252:255], v[144:147], v[92:95]\n"
  "s_sub_u32 s44, s44, 1\n"
  "v_cvt_pk_bf16_f32 v163, v166, v167\n"
  "s_waitcnt vmcnt(8) lgkmcnt(0)\n"
  "s_cbranch_scc0 L_loop2\n"
  "s_branch L_disp\n"
  "L_loop1:\n"
  "v_mfma_f32_16x16x32_bf16 v[128:131], a[192:195], a[0:3], 0\n"
  "s_barrier\n"
  "ds_read_b64_tr_b16 a[224:225], v221 offset:0\n"
  "ds_read_b64_tr_b16 a[226:227], v221 offset:4096\n"
  "v_mfma_f32_16x16x32_bf16 v[128:131], a[196:199], a[4:7], v[128:131]\n"
  "ds_read_b64_tr_b16 a[228:229], v239 offset:0\n"
  "ds_read_b64_tr_b16 a[230:231], v239 offset:4096\n"
  "ds_read_b64_tr_b16 a[232:233], v221 offset:512\n"
  "ds_read_b64_tr_b16 a[234:235], v221 offset:4608\n"
  "v_mfma_f32_16x16x32_bf16 v[128:131], a[200:203], a[8:11], v[128:131]\n"
  "buffer_store_dwordx2 v[232:233], v227, s[24:27], s51 offen sc1\n"
  "ds_read_b64_tr_b16 a[236:237], v239 offset:512\n"
  "v_mfma_f32_16x16x32_bf16 v[128:131], a[204:207], a[12:15], v[128:131]\n"
  "buffer_load_dwordx2 v[230:231], v227, s[24:27], s50 offen sc1\n"
  "s_or_b32 m0, s45, s43\n"
  "v_mfma_f32_16x16x32_bf16 v[132:135], a[208:211], a[0:3], 0\n"
  "buffer_load_dwordx4 v222, s[8:11], s39 offen offset:0 lds\n"
  "ds_read_b64_tr_b16 a[238:239], v239 offset:4608\n"
  "v_mfma_f32_16x16x32_bf16 v[132:135], a[212:215], a[4:7], v[132:135]\n"
  "ds_read_b64_tr_b16 a[240:241], v221 offset:1024\n"
  "ds_read_b64_tr_b16 a[242:243], v221 offset:5120\n"
  "ds_read_b64_tr_b16 a[244:245], v239 offset:1024\n"
  "ds_read_b64_tr_b16 a[246:247], v239 offset:5120\n"
  "ds_read_b64_tr_b16 a[248:249], v221 offset:1536\n"
  "ds_read_b64_tr_b16 a[250:251], v221 offset:5632\n"
  "ds_read_b64_tr_b16 a[252:253], v239 offset:1536\n"
  "ds_read_b64_tr_b16 a[254:255], v239 offset:5632\n"
  "v_mfma_f32_16x16x32_bf16 v[132:135], a[216:219], a[8:11], v[132:135]\n"
  "buffer_load_dwordx4 v223, s[8:11], s39 offen offset:1024 lds\n"
  "v_mfma_f32_16x16x32_bf16 v[132:135], a[220:223], a[12:15], v[132:135]\n"
  "s_waitcnt lgkmcnt(0)\n"
  "v_mfma_f32_16x16x32_bf16 v[192:195], v[216:219], v[160:163], v[192:195]\n"
  "buffer_load_dwordx4 v224, s[8:11], s39 offen offset:2048 lds\n"
  "ds_read_b128 a[192:195], v220 offset:0\n"
  "ds_read_b128 a[196:199], v220 offset:512\n"
  "ds_read_b128 a[200:203], v220 offset:1024\n"
  "v_mfma_f32_16x16x32_bf16 v[0:3], a[224:227], v[160:163], v[0:3]\n"
  "buffer_load_dwordx4 v225, s[8:11], s39 offen offset:3072 lds\n"
  "ds_read_b128 a[204:207], v220 offset:1536\n"
  "ds_read_b128 a[208:211], v220 offset:4096\n"
  "ds_read_b128 a[212:215], v220 offset:4608\n"
  "ds_read_b128 a[216:219], v220 offset:5120\n"
  "v_mfma_f32_16x16x32_bf16 v[4:7], a[228:231], v[160:163], v[4:7]\n"
  "s_or_b32 m0, s46, s43\n"
  "v_fma_f32 v128, v128, s38, v208\n"
  "v_fma_f32 v129, v129, s38, v208\n"
  "v_fma_f32 v130, v130, s38, v208\n"
  "v_fma_f32 v131, v131, s38, v208\n"
  "v_mfma_f32_16x16x32_bf16 v[8:11], a[232:235], v[160:163], v[8:11]\n"
  "buffer_load_dwordx4 v222, s[12:15], s41 offen offset:0 lds\n"
  "ds_read_b128 a[220:223], v220 offset:5632\n"
  "v_exp_f32 v128, v128\n"
  "v_fma_f32 v132, v132, s38, v208\n"
  "v_mfma_f32_16x16x32_bf16 v[12:15], a[236:239], v[160:163], v[12:15]\n"
  "buffer_load_dwordx4 v223, s[12:15], s41 offen offset:1024 lds\n"
  "v_exp_f32 v129, v129\n"
  "v_fma_f32 v133, v133, s38, v208\n"
  "v_mfma_f32_16x16x32_bf16 v[16:19], a[240:243], v[160:163], v[16:19]\n"
  "v_cvt_pk_bf16_f32 v128, v128, v129\n"
  "v_exp_f32 v130, v130\n"
  "v_exp_f32 v131, v131\n"
  "v_fma_f32 v134, v134, s38, v208\n"
  "v_mfma_f32_16x16x32_bf16 v[20:23], a[244:247], v[160:163], v[20:23]\n"
  "buffer_load_dwordx4 v224, s[12:15], s41 offen offset:2048 lds\n"
  "v_cvt_pk_bf16_f32 v129, v130, v131\n"
  "v_exp_f32 v132, v132\n"
  "v_mfma_f32_16x16x32_bf16 v[24:27], a[248:251], v[160:163], v[24:27]\n"
  "v_exp_f32 v133, v133\n"
  "v_exp_f32 v134, v134\n"
  "v_fma_f32 v135, v135, s38, v208\n"
  "v_mfma_f32_16x16x32_bf16 v[28:31], a[252:255], v[160:163], v[28:31]\n"
  "buffer_load_dwordx4 v225, s[12:15], s41 offen offset:3072 lds\n"
  "v_cvt_pk_bf16_f32 v130, v132, v133\n"
  "v_exp_f32 v135, v135\n"
  "s_waitcnt lgkmcnt(0)\n"
  "v_cvt_pk_bf16_f32 v131, v134, v135\n"
  "L_loop1_G2:\n"
  "v_mfma_f32_16x16x32_bf16 v[160:163], a[192:195], a[0:3], 0\n"
  "ds_read_b64_tr_b16 a[224:225], v221 offset:8192\n"
  "ds_read_b64_tr_b16 a[226:227], v221 offset:12288\n"
  "ds_read_b64_tr_b16 a[228:229], v239 offset:8192\n"
  "v_mfma_f32_16x16x32_bf16 v[160:163], a[196:199], a[4:7], v[160:163]\n"
  "ds_read_b64_tr_b16 a[230:231], v239 offset:12288\n"
  "ds_read_b64_tr_b16 a[232:233], v221 offset:8704\n"
  "v_mfma_f32_16x16x32_bf16 v[160:163], a[200:203], a[8:11], v[160:163]\n"
  "ds_read_b64_tr_b16 a[234:235], v221 offset:12800\n"
  "ds_read_b64_tr_b16 a[236:237], v239 offset:8704\n"
  "ds_read_b64_tr_b16 a[238:239], v239 offset:12800\n"
  "v_mfma_f32_16x16x32_bf16 v[160:163], a[204:207], a[12:15], v[160:163]\n"
  "ds_read_b64_tr_b16 a[240:241], v221 offset:9216\n"
  "ds_read_b64_tr_b16 a[242:243], v221 offset:13312\n"
  "v_mfma_f32_16x16x32_bf16 v[164:167], a[208:211], a[0:3], 0\n"
  "ds_read_b64_tr_b16 a[244:245], v239 offset:9216\n"
  "ds_read_b64_tr_b16 a[246:247], v239 offset:13312\n"
  "ds_read_b64_tr_b16 a[248:249], v221 offset:9728\n"
  "v_mfma_f32_16x16x32_bf16 v[164:167], a[212:215], a[4:7], v[164:167]\n"
  "ds_read_b64_tr_b16 a[250:251], v221 offset:13824\n"
  "ds_read_b64_tr_b16 a[252:253], v239 offset:9728\n"
  "ds_read_b64_tr_b16 a[254:255], v239 offset:13824\n"
  "v_mfma_f32_16x16x32_bf16 v[164:167], a[216:219], a[8:11], v[164:167]\n"
  "s_mov_b32 s41, s39\n"
  "s_mov_b32 s14, s10\n"
  "v_mfma_f32_16x16x32_bf16 v[164:167], a[220:223], a[12:15], v[164:167]\n"
  "s_add_u32 s39, s39, 0x40000\n"
  "v_xor_b32 v221, s43, v221\n"
  "v_xor_b32 v239, s43, v239\n"
  "s_waitcnt lgkmcnt(0)\n"
  "v_mfma_f32_16x16x32_bf16 v[192:195], v[212:215], v[128:131], v[192:195]\n"
  "ds_read_b128 a[192:195], v220 offset:8192\n"
  "ds_read_b128 a[196:199], v220 offset:8704\n"
  "ds_read_b128 a[200:203], v220 offset:9216\n"
  "ds_read_b128 a[204:207], v220 offset:9728\n"
  "v_mfma_f32_16x16x32_bf16 v[0:3], a[224:227], v[128:131], v[0:3]\n"
  "ds_read_b128 a[208:211], v220 offset:12288\n"
  "ds_read_b128 a[212:215], v220 offset:12800\n"
  "ds_read_b128 a[216:219], v220 offset:13312\n"
  "ds_read_b128 a[220:223], v220 offset:13824\n"
  "v_xor_b32 v220, s43, v220\n"
  "v_mfma_f32_16x16x32_bf16 v[4:7], a[228:231], v[128:131], v[4:7]\n"
  "v_fma_f32 v160, v160, s38, v208\n"
  "v_fma_f32 v161, v161, s38, v208\n"
  "v_fma_f32 v162, v162, s38, v208\n"
  "v_fma_f32 v163, v163, s38, v208\n"
  "v_mfma_f32_16x16x32_bf16 v[8:11], a[232:235], v[128:131], v[8:11]\n"
  "v_exp_f32 v160, v160\n"
  "v_exp_f32 v161, v161\n"
  "v_fma_f32 v164, v164, s38, v208\n"
  "v_mfma_f32_16x16x32_bf16 v[12:15], a[236:239], v[128:131], v[12:15]\n"
  "v_cvt_pk_bf16_f32 v160, v160, v161\n"
  "v_exp_f32 v162, v162\n"
  "v_fma_f32 v165, v165, s38, v208\n"
  "v_mfma_f32_16x16x32_bf16 v[16:19], a[240:243], v[128:131], v[16:19]\n"
  "v_exp_f32 v163, v163\n"
  "v_exp_f32 v164, v164\n"
  "v_fma_f32 v166, v166, s38, v208\n"
  "v_mfma_f32_16x16x32_bf16 v[20:23], a[244:247], v[128:131], v[20:23]\n"
  "s_xor_b32 s42, s42, s43\n"
  "v_cvt_pk_bf16_f32 v161, v162, v163\n"
  "v_exp_f32 v165, v165\n"
  "v_mfma_f32_16x16x32_bf16 v[24:27], a[248:251], v[128:131], v[24:27]\n"
  "s_xor_b32 s43, s42, s43\n"
  "v_cvt_pk_bf16_f32 v162, v164, v165\n"
  "v_exp_f32 v166, v166\n"
  "v_fma_f32 v167, v167, s38, v208\n"
  "v_mfma_f32_16x16x32_bf16 v[28:31], a[252:255], v[128:131], v[28:31]\n"
  "s_sub_u32 s44, s44, 1\n"
  "v_exp_f32 v167, v167\n"
  "s_waitcnt vmcnt(8) lgkmcnt(0)\n"
  "v_cvt_pk_bf16_f32 v163, v166, v167\n"
  "s_cbranch_scc0 L_loop1\n"
  "s_branch L_disp\n"
  "L_exit:\n"
    : "+{v220}"(rk0), "+{v221}"(rv0), "+{v239}"(rv1), "+{v228}"(tab),
    "+{s[8:11]}"(srdK), "+{s[12:15]}"(srdV), "+{s[16:19]}"(srdQ), "+{s[20:23]}"(srdO), "={s54}"(fail)
    : "{v222}"(dm[0]), "{v223}"(dm[1]), "{v224}"(dm[2]), "{v225}"(dm[3]), "{v229}"(ost), "{v226}"(lane16), "{v227}"(flagoff),
    "{s[24:27]}"(srdW), "{s53}"(w), "{s52}"(wrow), "{s55}"(nseg8), "{s[56:57]}"(tag), "{s82}"(stg)
    : "v0", "v1", "v2", "v3", "v4", "v5", "v6", "v7", "v8", "v9", "v10", "v11", "v12", "v13", "v14", "v15", "v16", "v17", "v18", "v19", "v20", "v21", "v22", "v23",
    "v24", "v25", "v26", "v27", "v28", "v29", "v30", "v31", "v32", "v33", "v34", "v35", "v36", "v37", "v38", "v39", "v40", "v41", "v42", "v43", "v44", "v45", "v46", "v47",
    "v48", "v49", "v50", "v51", "v52", "v53", "v54", "v55", "v56", "v57", "v58", "v59", "v60", "v61", "v62", "v63", "v64", "v65", "v66", "v67", "v68", "v69", "v70", "v71",
    "v72", "v73", "v74", "v75", "v76", "v77", "v78", "v79", "v80", "v81", "v82", "v83", "v84", "v85", "v86", "v87", "v88", "v89", "v90", "v91", "v92", "v93", "v94", "v95",
    "v96", "v97", "v98", "v99", "v100", "v101", "v102", "v103", "v104", "v105", "v106", "v107", "v108", "v109", "v110", "v111", "v112", "v113", "v114", "v115", "v116", "v117", "v118", "v119",
    "v120", "v121", "v122", "v123", "v124", "v125", "v126", "v127", "v128", "v129", "v130", "v131", "v132", "v133", "v134", "v135", "v136", "v137", "v138", "v139", "v140", "v141", "v142", "v143",
    "v144", "v145", "v146", "v147", "v148", "v149", "v150", "v151", "v152", "v153", "v154", "v155", "v156", "v157", "v158", "v159", "v160", "v161", "v162", "v163", "v164", "v165", "v166", "v167",
    "v168", "v169", "v170", "v171", "v172", "v173", "v174", "v175", "v176", "v177", "v178", "v179", "v180", "v181", "v182", "v183", "v184", "v185", "v186", "v187", "v188", "v189", "v190", "v191",
    "v192", "v193", "v194", "v195", "v196", "v197", "v198", "v199", "v200", "v201", "v202", "v203", "v204", "v205", "v206", "v207", "v208", "v209", "v210", "v211", "v212", "v213", "v214", "v215",
    "v216", "v217", "v218", "v219", "v230", "v231", "v232", "v233", "v234", "v235", "v236", "v237", "v238", "v240", "v241", "v242", "v243", "v244", "v245", "v246", "v247", "v248", "v249", "v250",
    "v251", "v252", "v253", "v254", "v255", "a0", "a1", "a2", "a3", "a4", "a5", "a6", "a7", "a8", "a9", "a10", "a11", "a12", "a13", "a14", "a15", "a16", "a17", "a18",
    "a19", "a20", "a21", "a22", "a23", "a24", "a25", "a26", "a27", "a28", "a29", "a30", "a31", "a32", "a33", "a34", "a35", "a36", "a37", "a38", "a39", "a40", "a41", "a42",
    "a43", "a44", "a45", "a46", "a47", "a48", "a49", "a50", "a51", "a52", "a53", "a54", "a55", "a56", "a57", "a58", "a59", "a60", "a61", "a62", "a63", "a64", "a65", "a66",
    "a67", "a68", "a69", "a70", "a71", "a72", "a73", "a74", "a75", "a76", "a77", "a78", "a79", "a80", "a81", "a82", "a83", "a84", "a85", "a86", "a87", "a88", "a89", "a90",
    "a91", "a92", "a93", "a94", "a95", "a96", "a97", "a98", "a99", "a100", "a101", "a102", "a103", "a104", "a105", "a106", "a107", "a108", "a109", "a110", "a111", "a112", "a113", "a114",
    "a115", "a116", "a117", "a118", "a119", "a120", "a121", "a122", "a123", "a124", "a125", "a126", "a127", "a128", "a129", "a130", "a131", "a132", "a133", "a134", "a135", "a136", "a137", "a138",
    "a139", "a140", "a141", "a142", "a143", "a144", "a145", "a146", "a147", "a148", "a149", "a150", "a151", "a152", "a153", "a154", "a155", "a156", "a157", "a158", "a159", "a160", "a161", "a162",
    "a163", "a164", "a165", "a166", "a167", "a168", "a169", "a170", "a171", "a172", "a173", "a174", "a175", "a176", "a177", "a178", "a179", "a180", "a181", "a182", "a183", "a184", "a185", "a186",
    "a187", "a188", "a189", "a190", "a191", "a192", "a193", "a194", "a195", "a196", "a197", "a198", "a199", "a200", "a201", "a202", "a203", "a204", "a205", "a206", "a207", "a208", "a209", "a210",
    "a211", "a212", "a213", "a214", "a215", "a216", "a217", "a218", "a219", "a220", "a221", "a222", "a223", "a224", "a225", "a226", "a227", "a228", "a229", "a230", "a231", "a232", "a233", "a234",
    "a235", "a236", "a237", "a238", "a239", "a240", "a241", "a242", "a243", "a244", "a245", "a246", "a247", "a248", "a249", "a250", "a251", "a252", "a253", "a254", "a255", "s28", "s29", "s30",
    "s31", "s34", "s35", "s36", "s37", "s38", "s39", "s40", "s41", "s42", "s43", "s44", "s45", "s46", "s47", "s48", "s49", "s50", "s51", "s58", "s59", "s60", "s61", "s62",
    "s63", "s64", "s65", "s66", "s67", "s68", "s69", "s70", "s71", "s72", "s73", "s74", "s75", "s76", "s77", "s78", "s79", "s80", "s81", "s83", "s87", "s88", "vcc", "scc",
    "memory");
  }
  if (__builtin_expect(fail != 0 || rest != 0, 0)) {
    asm volatile("s_waitcnt vmcnt(0) lgkmcnt(0)" ::: "memory");
    unsigned sgi = 0;
    for (;;) {
      unsigned ro, L, qq, md;
      if (sgi < nseg) { const unsigned b = sgi * 8; ++sgi; if (!((fail >> (sgi - 1)) & 1)) continue;
        ro = rdl(tab, b + 4); L = (rdl(tab, b + 1) - 256u - ro) / ROWB; qq = (rdl(tab, b) - ro) / ROWB; md = rdl(tab, b + 3) >> 16; }
      else if (rest) { const unsigned it = __builtin_ctzll(rest); rest &= rest - 1; ArgsP kb = (ArgsP)__builtin_amdgcn_kernarg_segment_ptr(); asm volatile("" : "+s"(kb));
        const unsigned i = it >> kb->lgw; L = (unsigned)(kb->cu[i + 1] - kb->cu[i]); ro = (unsigned)kb->cu[i] * ROWB; md = 4u; qq = 256u * (it & ((1u << kb->lgw) - 1u)); }
      else break;
      const int row0 = (int)(qq + w * 16 * md), rend = row0 + 16 * (int)md;
      for (int rr = row0; rr < rend && rr < (int)L; rr += 32) slow_rows(srd_base(srdQ) + ro, srd_base(srdK) + ro, srd_base(srdV) + ro, srd_base(srdO) + ro, (int)L, rr, rend, lds + stg);
    }
  }
}

__global__ __launch_bounds__(256) void varlen_attn_generic(Args a) {
  __shared__ __attribute__((aligned(16))) char st[4 * 8192];
  const unsigned w = rfl(threadIdx.x >> 6), hd = blockIdx.y; unsigned it = blockIdx.x * 4 + w;
  for (int i = 0; i < a.nseq; ++i) { const int s0 = a.cu[i], L = a.cu[i + 1] - s0; const unsigned nb = (unsigned)(L + 31) >> 5;
    if (it < nb) { const size_t ro = (size_t)s0 * ROWB + hd * 256; slow_rows(a.Q + ro, a.K + ro, a.V + ro, a.O + ro, L, (int)it * 32, L, st + w * 8192); return; }
    it -= nb; }
}

extern "C" void kernel_launch(void* const* d_in, const int* in_sizes, int n_in, void* d_out, int out_size, void* d_ws, size_t ws_size, hipStream_t stream) {
  Args a; a.Q = (const char*)d_in[0]; a.K = (const char*)d_in[1]; a.V = (const char*)d_in[2]; a.cu = (const int*)d_in[3]; a.O = (char*)d_out; a.WS = (char*)d_ws;
  a.nseq = in_sizes[3] - 1; a.ntok = in_sizes[0] / (NH * HD);
  if (a.nseq < 1 || a.ntok < 1) return;
  a.lgw = 0; a.pad0 = 0;
  while ((256ll << a.lgw) < a.ntok) ++a.lgw;
  if (a.lgw <= 6 && ((long long)a.nseq << a.lgw) <= 64 && ws_size >= (size_t)256 * PSTRIDE) {
    (void)hipFuncSetAttribute((const void*)varlen_attn_fwd, hipFuncAttributeMaxDynamicSharedMemorySize, (int)LDS_BYTES);
    hipLaunchKernelGGL(varlen_attn_fwd, dim3(NH * GPH), dim3(256), LDS_BYTES, stream, a);
    return;
  }
  hipLaunchKernelGGL(varlen_attn_generic, dim3((a.ntok / 32 + a.nseq + 3) / 4, NH), dim3(256), 0, stream, a);
}
```
